```python
import torch

def _softmax(x, axis=-1):
    return torch.softmax(x, dim=axis)
def _gelu(x):
    return torch.nn.functional.gelu(x, approximate="tanh")
def _silu(x):
    return torch.nn.functional.silu(x)
def _sigmoid(x):
    return torch.sigmoid(x)
def _layernorm(x, axis=-1, eps=1e-5):
    m = x.mean(dim=axis, keepdim=True)
    v = x.var(dim=axis, keepdim=True, unbiased=False)
    return (x - m) / torch.sqrt(v + eps)
def _scatter_add(tgt, idx, src, axis=0):
    return tgt.clone().index_add_(axis, idx, src)
def _segment_sum(src, seg, n):
    out = torch.zeros((n,) + src.shape[1:], dtype=src.dtype)
    return out.index_add_(0, seg, src)
def _pad(x, pw):
    flat = tuple(v for pair in reversed(pw) for v in pair)
    return torch.nn.functional.pad(x, flat)

kernel_name = 'parallel_sum__mha_block__dense_activation_relu__prompt'

def setup_inputs():

    _w = (lambda a: (lambda t: t.to(torch.bfloat16) if t.is_floating_point() else t)(torch.as_tensor(a)))
    def _A_setup():
        B, S, D, H = 1, 2048, 512, 32
        return {
            "x": _w((lambda *s: torch.randn(*s, dtype=torch.bfloat16) * 0.05)(B, S, D)),
            "wqkv": _w((lambda *s: torch.randn(*s, dtype=torch.bfloat16) * 0.2)(D, 3 * D)),
            "wo": _w((lambda *s: torch.randn(*s, dtype=torch.bfloat16) * 0.1)(D, D)),
        }
    _A_in = _A_setup()

    def _B_setup():
        B, S, D, Dff = 1, 2048, 512, 1792
        return {
            "x": _w((lambda *s: torch.randn(*s, dtype=torch.bfloat16) * 0.05)(B, S, D)),
            "w1": _w((lambda *s: torch.randn(*s, dtype=torch.bfloat16) * 0.2)(D, Dff)),
            "w2": _w((lambda *s: torch.randn(*s, dtype=torch.bfloat16) * 0.2)(Dff, D)),
        }
    _B_in = _B_setup()

    return {
        "A_x": _A_in["x"],
        "A_wqkv": _A_in["wqkv"],
        "A_wo": _A_in["wo"],
        "B_x": _B_in["x"],
        "B_w1": _B_in["w1"],
        "B_w2": _B_in["w2"],
    }

def reference(A_x, A_wqkv, A_wo, B_x, B_w1, B_w2):
    def _A_ref(x, wqkv, wo):
        B, S, D = x.shape
        H = 32
        Dh = D // H
        qkv = (lambda x, *d: x.permute(*d))((x @ wqkv).reshape(B, S, 3, H, Dh), 2, 0, 3, 1, 4)
        q, k, v = qkv[0], qkv[1], qkv[2]
        scores = torch.einsum("bhqd,bhkd->bhqk", q, k) * (Dh ** -0.5)
        p = _softmax(scores, axis=-1)
        o = torch.einsum("bhqk,bhkd->bhqd", p, v)
        return (lambda x, *d: x.permute(*d))(o, 0, 2, 1, 3).reshape(B, S, D) @ wo

    def _B_ref(x, w1, w2):
        h = torch.einsum("bsd,df->bsf", x, w1)
        zero = torch.zeros_like(h)
        h = torch.maximum(h, zero)
        return torch.einsum("bsf,fd->bsd", h, w2)

    x = A_x
    xn = _layernorm(x, axis=-1)
    return x + _A_ref(xn, A_wqkv, A_wo) + _B_ref(xn, B_w1, B_w2)
```

```cpp
#include <hip/hip_runtime.h>
#include <hip/hip_bf16.h>
#include <stdint.h>

typedef short bf16x8 __attribute__((ext_vector_type(8)));
typedef short s16x4 __attribute__((ext_vector_type(4)));
typedef float f32x16 __attribute__((ext_vector_type(16)));
typedef float f32x4 __attribute__((ext_vector_type(4)));
typedef float f32x2 __attribute__((ext_vector_type(2)));
typedef unsigned u32x4 __attribute__((ext_vector_type(4)));
typedef unsigned u32x2 __attribute__((ext_vector_type(2)));
#define LAS __attribute__((address_space(3)))
#define DI __device__ __forceinline__
#define MFMA(a, b, c) __builtin_amdgcn_mfma_f32_32x32x16_bf16((a), (b), (c), 0, 0, 0)
#define SBAR() __builtin_amdgcn_sched_barrier(0)

constexpr int SEQ = 2048, DM = 512, NH = 32, DFF = 1792;
constexpr int VT_BYTES = 1088;
constexpr size_t QF_OFF = 0;
constexpr size_t KF_OFF = QF_OFF + (size_t)NH * 64 * 1024;
constexpr size_t VF_OFF = KF_OFF + (size_t)NH * 64 * 1024;
constexpr size_t XN_OFF = VF_OFF + (size_t)NH * 64 * VT_BYTES;
constexpr size_t W1F_OFF = XN_OFF + (size_t)64 * 32 * 1024;
constexpr size_t W2F_OFF = W1F_OFF + (size_t)56 * 32 * 1024;
constexpr size_t WOF_OFF = W2F_OFF + (size_t)16 * 112 * 1024;
constexpr size_t HF_OFF = WOF_OFF + (size_t)16 * 32 * 1024;
constexpr size_t OF_OFF = HF_OFF + (size_t)64 * 112 * 1024;
constexpr size_t F2_OFF = OF_OFF + (size_t)64 * 32 * 1024;
constexpr size_t WS_END = F2_OFF + (size_t)SEQ * DM * 2;

DI unsigned cvtpk(float lo, float hi) {
  typedef __bf16 bf2 __attribute__((ext_vector_type(2)));
  f32x2 v = {lo, hi};
  bf2 r = __builtin_convertvector(v, bf2);
  return __builtin_bit_cast(unsigned, r);
}
DI float bf_lo(unsigned p) { return __uint_as_float(p << 16); }
DI float bf_hi(unsigned p) { return __uint_as_float(p & 0xffff0000u); }
DI float rbf(float x) { return bf_lo(cvtpk(x, 0.f)); }

DI void glds16(unsigned voff, const void* sbase, unsigned ldsbase) {
  unsigned sv;
  asm volatile("s_mov_b32 %0, m0\n\ts_mov_b32 m0, %3\n\ts_nop 0\n\tglobal_load_lds_dwordx4 %1, %2\n\ts_mov_b32 m0, %0"
               : "=&s"(sv) : "v"(voff), "s"(sbase), "s"(ldsbase) : "memory");
}
typedef short v4i16_t __attribute__((ext_vector_type(4)));
DI s16x4 trrd(const LAS char* p) { return __builtin_bit_cast(s16x4, __builtin_amdgcn_ds_read_tr16_b64_v4i16((LAS v4i16_t*)p)); }
DI bf16x8 cat(s16x4 lo, s16x4 hi) { return __builtin_shufflevector(lo, hi, 0, 1, 2, 3, 4, 5, 6, 7); }
DI bf16x8 lds128(const LAS char* p) { return *(const LAS bf16x8*)p; }
#define WAITBAR(N) asm volatile("s_waitcnt vmcnt(" #N ") lgkmcnt(0)\n\ts_barrier" ::: "memory")

constexpr int K1_SLOT = 28672, K1_NSLOT = 5, K1_SCR = K1_SLOT * K1_NSLOT, K1_LDS = K1_SCR + 8 * 2048;

__global__ __launch_bounds__(512, 2) void k1_qkv(const char* __restrict__ x, const char* __restrict__ wqkv, const char* __restrict__ wo,
                                                 const char* __restrict__ w1, const char* __restrict__ w2, char* __restrict__ ws) {
  extern __shared__ __attribute__((aligned(16))) char smem[];
  LAS char* lds = (LAS char*)smem;
  const unsigned lds0 = (unsigned)(size_t)lds;
  const int tid = threadIdx.x, lane = tid & 63, r = lane & 31, h = lane >> 5;
  const int w = __builtin_amdgcn_readfirstlane(tid >> 6), wi = w & 3, wj = w >> 2;
  const int b = blockIdx.x, xcd = b & 7, idx = b >> 3;
  const int rp = (xcd >> 1) * 4 + (idx >> 3), p = (xcd & 1) * 8 + (idx & 7);

  unsigned voff[4];
  const char* sbase;
  unsigned ldst;
  int sstep;
  if (wj == 0) {
#pragma unroll
    for (int e = 0; e < 4; ++e) {
      const int row = 8 * (4 * wi + e) + (lane >> 3);
      voff[e] = row * 1024 + (((lane & 7) ^ ((row >> 1) & 7)) << 4);
    }
    sbase = x + (size_t)rp * 128 * 1024;
    sstep = 128;
    ldst = lds0 + wi * 4096;
  } else {
#pragma unroll
    for (int e = 0; e < 3; ++e) {
      const int o = 1024 * (3 * wi + e) + 16 * lane, row = o / 192, rem = o - row * 192;
      voff[e] = row * 3072 + (rem >> 6) * 1024 + (rem & 63);
    }
    voff[3] = 0;
    sbase = wqkv + p * 64;
    sstep = 64 * 3072;
    ldst = lds0 + 16384 + wi * 3072;
  }
  auto issue = [&](int c) {
    const char* sb = sbase + (size_t)c * sstep;
    const unsigned d = ldst + (c % K1_NSLOT) * K1_SLOT;
    glds16(voff[0], sb, d); glds16(voff[1], sb, d + 1024); glds16(voff[2], sb, d + 2048);
    if (wj == 0) glds16(voff[3], sb, d + 3072);
  };
#pragma unroll
  for (int c = 0; c < K1_NSLOT; ++c) issue(c);

  const int job = b * 8 + w;
  const char* rsrc; char* rdst; int rpitch, rperm, rdstep;
  if (job < 896) { const int cp = job >> 5, ks = job & 31; rsrc = w1 + (size_t)ks * 16 * (DFF * 2) + cp * 128; rpitch = DFF * 2; rperm = 0;
    rdst = ws + W1F_OFF + ((size_t)(2 * cp) * 32 + ks) * 1024; rdstep = 32 * 1024; }
  else if (job < 1792) { const int jj = job - 896, cp = jj / 112, ks = jj - cp * 112; rsrc = w2 + (size_t)ks * 16 * (DM * 2) + cp * 128; rpitch = DM * 2; rperm = 1;
    rdst = ws + W2F_OFF + ((size_t)(2 * cp) * 112 + ks) * 1024; rdstep = 112 * 1024; }
  else { const int jj = job - 1792, cp = jj >> 5, ks = jj & 31; rsrc = wo + (size_t)ks * 16 * (DM * 2) + cp * 128; rpitch = DM * 2; rperm = 1;
    rdst = ws + WOF_OFF + ((size_t)(2 * cp) * 32 + ks) * 1024; rdstep = 32 * 1024; }

  const int i16 = lane & 15, q4 = i16 >> 2, p4 = i16 & 3, nh = (lane >> 4) & 1;
  const int aoff0 = (32 * wi + r) * 128 + ((((4 * wj + 0) + h) ^ ((r >> 1) & 7)) << 4);
  const int aoff1 = (32 * wi + r) * 128 + ((((4 * wj + 2) + h) ^ ((r >> 1) & 7)) << 4);
  const int boff = 16384 + (32 * wj + 8 * h + q4) * 192 + nh * 32 + p4 * 8;

  f32x16 accq = {}, acck = {}, accv = {}, accc = {};
  f32x2 sx = {0.f, 0.f}, sxx = {0.f, 0.f};
  bf16x8 ones;
#pragma unroll
  for (int e = 0; e < 8; ++e) ones[e] = 0x3f80;
  bf16x8 sv0 = ones, sv1 = ones;

#pragma unroll
  for (int c = 0; c < 8; ++c) {
    if (wj == 0) {
      if (c == 0) WAITBAR(16); else if (c <= 3) WAITBAR(12); else if (c == 4) WAITBAR(14); else if (c == 5) WAITBAR(10); else if (c == 6) WAITBAR(6); else WAITBAR(2);
    } else {
      if (c == 0) WAITBAR(12); else if (c <= 3) WAITBAR(9); else if (c == 4) WAITBAR(11); else if (c == 5) WAITBAR(8); else if (c == 6) WAITBAR(5); else WAITBAR(2);
    }
    if (c >= 1 && c <= 3) issue(c + 4);
    if (c == 3) {
      const unsigned vo = (lane >> 3) * rpitch + (lane & 7) * 16;
      glds16(vo, rsrc, lds0 + K1_SCR + w * 2048);
      glds16(vo, rsrc + 8 * (size_t)rpitch, lds0 + K1_SCR + w * 2048 + 1024);
    }
    const LAS char* sl = lds + (c % K1_NSLOT) * K1_SLOT;
#pragma unroll
    for (int e = 0; e < 2; ++e) {
      const bf16x8 a = lds128(sl + (e ? aoff1 : aoff0));
      const LAS char* bp = sl + boff + e * 16 * 192;
      const bf16x8 bq = cat(trrd(bp), trrd(bp + 4 * 192));
      const bf16x8 bk = cat(trrd(bp + 64), trrd(bp + 64 + 4 * 192));
      const bf16x8 bv = cat(trrd(bp + 128), trrd(bp + 128 + 4 * 192));
      accq = MFMA(bq, a, accq);
      acck = MFMA(bk, a, acck);
      accv = MFMA(a, bv, accv);
      if (wi == 0) accc = MFMA(ones, bq, accc); else if (wi == 1) accc = MFMA(ones, bk, accc); else if (wi == 2) accc = MFMA(ones, bv, accc);
      const u32x4 au = __builtin_bit_cast(u32x4, a);
#pragma unroll
      for (int t = 0; t < 4; ++t) { const f32x2 v = {bf_lo(au[t]), bf_hi(au[t])}; sx += v; sxx += v * v; }
      if (c == (p >> 1) && wj == (p & 1)) { if (e == 0) sv0 = a; else sv1 = a; }
    }
  }
  WAITBAR(0);

  {
    LAS f32x4* ex = (LAS f32x4*)(lds + w * 6144) + lane;
    const int o = 8 * (1 - wj);
#pragma unroll
    for (int g = 0; g < 2; ++g) {
      f32x4 vq, vk, vv;
#pragma unroll
      for (int t = 0; t < 4; ++t) {
        vq[t] = wj ? accq[4 * g + t] : accq[8 + 4 * g + t];
        vk[t] = wj ? acck[4 * g + t] : acck[8 + 4 * g + t];
        vv[t] = wj ? accv[4 * g + t] : accv[8 + 4 * g + t];
      }
      ex[(0 + g) * 64] = vq; ex[(2 + g) * 64] = vk; ex[(4 + g) * 64] = vv;
    }
    (void)o;
    LAS f32x2* st = (LAS f32x2*)(lds + 49152);
    st[((wj * 4 + wi) * 2 + h) * 32 + r] = f32x2{sx[0] + sx[1], sxx[0] + sxx[1]};
    LAS float* cs = (LAS float*)(lds + 49152 + 4096);
    if (wi < 3 && h == 0) cs[(wj * 3 + wi) * 32 + r] = accc[0];
  }
  {
    const LAS char* sp = lds + K1_SCR + w * 2048 + nh * 32 + p4 * 8;
    const int r0 = rperm ? (4 * h + q4) : (8 * h + q4), r1 = rperm ? (8 + 4 * h + q4) : (8 * h + 4 + q4);
#pragma unroll
    for (int t = 0; t < 2; ++t) {
      const bf16x8 f = cat(trrd(sp + r0 * 128 + t * 64), trrd(sp + r1 * 128 + t * 64));
      *(bf16x8*)(rdst + (size_t)t * rdstep + lane * 16) = f;
    }
  }
  asm volatile("s_waitcnt lgkmcnt(0)\n\ts_barrier" ::: "memory");
  float q8[8], k8[8], v8[8];
  {
    const LAS f32x4* ex = (const LAS f32x4*)(lds + (w ^ 4) * 6144) + lane;
#pragma unroll
    for (int g = 0; g < 2; ++g) {
      const f32x4 vq = ex[(0 + g) * 64], vk = ex[(2 + g) * 64], vv = ex[(4 + g) * 64];
#pragma unroll
      for (int t = 0; t < 4; ++t) {
        q8[4 * g + t] = vq[t] + (wj ? accq[8 + 4 * g + t] : accq[4 * g + t]);
        k8[4 * g + t] = vk[t] + (wj ? acck[8 + 4 * g + t] : acck[4 * g + t]);
        v8[4 * g + t] = vv[t] + (wj ? accv[8 + 4 * g + t] : accv[4 * g + t]);
      }
    }
  }
  float mb, sb, rinv;
  {
    const LAS f32x2* st = (const LAS f32x2*)(lds + 49152);
    const f32x2 s0 = st[((0 + wi) * 2 + 0) * 32 + r], s1 = st[((0 + wi) * 2 + 1) * 32 + r], s2 = st[((4 + wi) * 2 + 0) * 32 + r], s3 = st[((4 + wi) * 2 + 1) * 32 + r];
    const float tx = (s0[0] + s1[0]) + (s2[0] + s3[0]), txx = (s0[1] + s1[1]) + (s2[1] + s3[1]);
    const float mean = tx * (1.f / DM);
    const float var = fmaxf(txx * (1.f / DM) - mean * mean, 0.f);
    mb = rbf(mean);
    sb = rbf(sqrtf(rbf(rbf(var) + 1e-5f)));
    rinv = 1.f / sb;
  }
  LAS f32x2* fin = (LAS f32x2*)(lds + 49152 + 8192) + w * 32;
  if (h == 0) fin[r] = f32x2{mb, rinv};
  const LAS float* cs = (const LAS float*)(lds + 49152 + 4096);
  const int rt = rp * 4 + wi;
  {
    const int f0 = 16 * wj + 4 * h;
    float cq[8], ck[8];
#pragma unroll
    for (int g = 0; g < 2; ++g) {
      const f32x4 a0 = *(const LAS f32x4*)(cs + 0 * 32 + f0 + 8 * g), a1 = *(const LAS f32x4*)(cs + 3 * 32 + f0 + 8 * g);
      const f32x4 b0 = *(const LAS f32x4*)(cs + 1 * 32 + f0 + 8 * g), b1 = *(const LAS f32x4*)(cs + 4 * 32 + f0 + 8 * g);
#pragma unroll
      for (int t = 0; t < 4; ++t) { cq[4 * g + t] = a0[t] + a1[t]; ck[4 * g + t] = b0[t] + b1[t]; }
    }
    const float rq = rinv * (0.25f * 1.4426950408889634f);
    u32x4 oq, ok;
#pragma unroll
    for (int t = 0; t < 4; ++t) {
      oq[t] = cvtpk((q8[2 * t] - mb * cq[2 * t]) * rq, (q8[2 * t + 1] - mb * cq[2 * t + 1]) * rq);
      ok[t] = cvtpk((k8[2 * t] - mb * ck[2 * t]) * rinv, (k8[2 * t + 1] - mb * ck[2 * t + 1]) * rinv);
    }
    const size_t off = ((size_t)(2 * p + wj) * 64 + rt) * 1024 + lane * 16;
    *(u32x4*)(ws + QF_OFF + off) = oq;
    *(u32x4*)(ws + KF_OFF + off) = ok;
  }
  {
    const float cv = cs[2 * 32 + r] + cs[5 * 32 + r];
    asm volatile("s_waitcnt lgkmcnt(0)" ::: "memory");
    float vv[8];
#pragma unroll
    for (int g = 0; g < 2; ++g)
#pragma unroll
      for (int t = 0; t < 4; ++t) { const f32x2 s = fin[16 * wj + 8 * g + 4 * h + t]; vv[4 * g + t] = (v8[4 * g + t] - s[0] * cv) * s[1]; }
    u32x4 ov;
#pragma unroll
    for (int t = 0; t < 4; ++t) ov[t] = cvtpk(vv[2 * t], vv[2 * t + 1]);
    char* vb = ws + VF_OFF + ((size_t)(2 * p + (r >> 4)) * 64 + rt) * VT_BYTES + (wj * 2 + h) * 272;
    *(u32x4*)(vb + (r & 15) * 16) = ov;
    if ((r & 15) == 0) *(u32x4*)(vb + 256) = u32x4{0x3f803f80u, 0x3f803f80u, 0x3f803f80u, 0x3f803f80u};
  }
  if (wj == (p & 1)) {
#pragma unroll
    for (int e = 0; e < 2; ++e) {
      const u32x4 au = __builtin_bit_cast(u32x4, e ? sv1 : sv0);
      u32x4 o;
#pragma unroll
      for (int t = 0; t < 4; ++t) {
        const unsigned d = cvtpk(bf_lo(au[t]) - mb, bf_hi(au[t]) - mb);
        o[t] = cvtpk(bf_lo(d) / sb, bf_hi(d) / sb);
      }
      *(u32x4*)(ws + XN_OFF + ((size_t)rt * 32 + 2 * p + e) * 1024 + lane * 16) = o;
    }
  }
}

template <int NC>
DI void ffn1_wave(char* __restrict__ ws, int rt0, int ct0, int lane) {
  const char* ap = ws + XN_OFF + (size_t)rt0 * 32 * 1024 + lane * 16;
  const char* bp = ws + W1F_OFF + (size_t)ct0 * 32 * 1024 + lane * 16;
  constexpr int PF = 4;
  bf16x8 a[PF + 1][2], bb[PF + 1][NC];
  f32x16 acc[2][NC];
#pragma unroll
  for (int i = 0; i < 2; ++i)
#pragma unroll
    for (int j = 0; j < NC; ++j) acc[i][j] = f32x16{};
#pragma unroll
  for (int k = 0; k < 32 + PF; ++k) {
    if (k < 32) {
      a[k % (PF + 1)][0] = *(const bf16x8*)(ap + k * 1024);
      a[k % (PF + 1)][1] = *(const bf16x8*)(ap + 32 * 1024 + k * 1024);
#pragma unroll
      for (int j = 0; j < NC; ++j) bb[k % (PF + 1)][j] = *(const bf16x8*)(bp + j * 32 * 1024 + k * 1024);
    }
    if (k >= PF) {
      const int s = (k - PF) % (PF + 1);
#pragma unroll
      for (int i = 0; i < 2; ++i)
#pragma unroll
        for (int j = 0; j < NC; ++j) acc[i][j] = MFMA(bb[s][j], a[s][i], acc[i][j]);
    }
    SBAR();
  }
#pragma unroll
  for (int i = 0; i < 2; ++i)
#pragma unroll
    for (int j = 0; j < NC; ++j)
#pragma unroll
      for (int s = 0; s < 2; ++s) {
        u32x4 o;
#pragma unroll
        for (int t = 0; t < 4; ++t) o[t] = cvtpk(fmaxf(acc[i][j][8 * s + 2 * t], 0.f), fmaxf(acc[i][j][8 * s + 2 * t + 1], 0.f));
        *(u32x4*)(ws + HF_OFF + ((size_t)(rt0 + i) * 112 + 2 * (ct0 + j) + s) * 1024 + lane * 16) = o;
      }
}

__global__ __launch_bounds__(256, 2) void k_ffn1(char* __restrict__ ws) {
  const int lane = threadIdx.x & 63, w = __builtin_amdgcn_readfirstlane(threadIdx.x >> 6);
  const int b = blockIdx.x, xcd = b & 7, idx = b >> 3;
  const int rp = xcd * 4 + (idx >> 3), cg = idx & 7;
  if (w < 3) ffn1_wave<2>(ws, 2 * rp, 7 * cg + 2 * w, lane);
  else ffn1_wave<1>(ws, 2 * rp, 7 * cg + 6, lane);
}

template <int NK, int PF>
DI void gemm22(const char* ap, size_t astride, const char* bp, size_t bstride, f32x16 (&acc)[2][2]) {
  bf16x8 a[PF + 1][2], bb[PF + 1][2];
#pragma unroll
  for (int k = 0; k < NK + PF; ++k) {
    if (k < NK) {
      a[k % (PF + 1)][0] = *(const bf16x8*)(ap + k * 1024);
      a[k % (PF + 1)][1] = *(const bf16x8*)(ap + astride + k * 1024);
      bb[k % (PF + 1)][0] = *(const bf16x8*)(bp + k * 1024);
      bb[k % (PF + 1)][1] = *(const bf16x8*)(bp + bstride + k * 1024);
    }
    if (k >= PF) {
      const int s = (k - PF) % (PF + 1);
#pragma unroll
      for (int i = 0; i < 2; ++i)
#pragma unroll
        for (int j = 0; j < 2; ++j) acc[i][j] = MFMA(bb[s][j], a[s][i], acc[i][j]);
    }
    SBAR();
  }
}
DI void dump22(LAS char* img, const f32x16 (&acc)[2][2], int r, int h) {
#pragma unroll
  for (int i = 0; i < 2; ++i)
#pragma unroll
    for (int j = 0; j < 2; ++j)
#pragma unroll
      for (int g = 0; g < 4; ++g)
        *(LAS f32x4*)(img + (32 * i + r) * 272 + (32 * j + 8 * g + 4 * h) * 4) = f32x4{acc[i][j][4 * g], acc[i][j][4 * g + 1], acc[i][j][4 * g + 2], acc[i][j][4 * g + 3]};
}

__global__ __launch_bounds__(256, 2) void k_ffn2(char* __restrict__ ws) {
  extern __shared__ __attribute__((aligned(16))) char smem[];
  LAS char* lds = (LAS char*)smem;
  const int tid = threadIdx.x, lane = tid & 63, r = lane & 31, h = lane >> 5, w = __builtin_amdgcn_readfirstlane(tid >> 6);
  const int b = blockIdx.x, xcd = b & 7, idx = b >> 3;
  const int rp = xcd * 4 + (idx >> 3), cg = idx & 7;
  f32x16 acc[2][2] = {};
  gemm22<28, 4>(ws + HF_OFF + ((size_t)(2 * rp) * 112 + 28 * w) * 1024 + lane * 16, 112 * 1024,
                ws + W2F_OFF + ((size_t)(2 * cg) * 112 + 28 * w) * 1024 + lane * 16, 112 * 1024, acc);
  dump22(lds + w * 17408, acc, r, h);
  __syncthreads();
#pragma unroll
  for (int e = 0; e < 2; ++e) {
    const int row = e * 32 + (tid >> 3), c8 = tid & 7;
    f32x4 s0 = {}, s1 = {};
#pragma unroll
    for (int u = 0; u < 4; ++u) { const LAS char* p = lds + u * 17408 + row * 272 + c8 * 32; s0 += *(const LAS f32x4*)p; s1 += *(const LAS f32x4*)(p + 16); }
    *(u32x4*)(ws + F2_OFF + (size_t)(rp * 64 + row) * 1024 + cg * 128 + c8 * 16) = u32x4{cvtpk(s0[0], s0[1]), cvtpk(s0[2], s0[3]), cvtpk(s1[0], s1[1]), cvtpk(s1[2], s1[3])};
  }
}

__global__ __launch_bounds__(256, 2) void k3_out(const char* __restrict__ x, const char* __restrict__ ws, char* __restrict__ out) {
  extern __shared__ __attribute__((aligned(16))) char smem[];
  LAS char* lds = (LAS char*)smem;
  const int tid = threadIdx.x, lane = tid & 63, r = lane & 31, h = lane >> 5, w = __builtin_amdgcn_readfirstlane(tid >> 6);
  const int b = blockIdx.x, xcd = b & 7, idx = b >> 3;
  const int rp = xcd * 4 + (idx >> 3), cg = idx & 7;
  u32x4 xv[2], fv[2];
#pragma unroll
  for (int e = 0; e < 2; ++e) {
    const size_t o = (size_t)(rp * 64 + e * 32 + (tid >> 3)) * 1024 + cg * 128 + (tid & 7) * 16;
    xv[e] = *(const u32x4*)(x + o); fv[e] = *(const u32x4*)(ws + F2_OFF + o);
  }
  f32x16 acc[2][2] = {};
  gemm22<8, 8>(ws + OF_OFF + ((size_t)(2 * rp) * 32 + 8 * w) * 1024 + lane * 16, 32 * 1024,
               ws + WOF_OFF + ((size_t)(2 * cg) * 32 + 8 * w) * 1024 + lane * 16, 32 * 1024, acc);
  dump22(lds + w * 17408, acc, r, h);
  __syncthreads();
#pragma unroll
  for (int e = 0; e < 2; ++e) {
    const int row = e * 32 + (tid >> 3), c8 = tid & 7;
    f32x4 s0 = {}, s1 = {};
#pragma unroll
    for (int u = 0; u < 4; ++u) { const LAS char* p = lds + u * 17408 + row * 272 + c8 * 32; s0 += *(const LAS f32x4*)p; s1 += *(const LAS f32x4*)(p + 16); }
    const float a[8] = {s0[0], s0[1], s0[2], s0[3], s1[0], s1[1], s1[2], s1[3]};
    u32x4 o;
#pragma unroll
    for (int t = 0; t < 4; ++t) {
      const unsigned at = cvtpk(a[2 * t], a[2 * t + 1]);
      const unsigned s = cvtpk(bf_lo(xv[e][t]) + bf_lo(at), bf_hi(xv[e][t]) + bf_hi(at));
      o[t] = cvtpk(bf_lo(s) + bf_lo(fv[e][t]), bf_hi(s) + bf_hi(fv[e][t]));
    }
    *(u32x4*)(out + (size_t)(rp * 64 + row) * 1024 + cg * 128 + c8 * 16) = o;
  }
}

constexpr int KV_K = 0, KV_V = 65536, KV_BYTES = 65536 + 64 * VT_BYTES;
constexpr float M_OFFSET = 90.f;

DI float max16(const f32x16& s) {
  float a = fmaxf(fmaxf(s[0], s[1]), s[2]), c = fmaxf(fmaxf(s[3], s[4]), s[5]);
  a = fmaxf(fmaxf(a, s[6]), s[7]); c = fmaxf(fmaxf(c, s[8]), s[9]);
  a = fmaxf(fmaxf(a, s[10]), s[11]); c = fmaxf(fmaxf(c, s[12]), s[13]);
  a = fmaxf(fmaxf(a, s[14]), s[15]);
  return fmaxf(a, c);
}
DI float xhalf_max(float m) {
  auto rr = __builtin_amdgcn_permlane32_swap(__float_as_uint(m), __float_as_uint(m), false, false);
  return fmaxf(__uint_as_float(rr[0]), __uint_as_float(rr[1]));
}
DI bf16x8 pack8(const f32x16& s, int o) {
  u32x4 p = {cvtpk(s[o], s[o + 1]), cvtpk(s[o + 2], s[o + 3]), cvtpk(s[o + 4], s[o + 5]), cvtpk(s[o + 6], s[o + 7])};
  return __builtin_bit_cast(bf16x8, p);
}
__device__ __noinline__ f32x16 attn_slow(const LAS char* kp, const LAS char* vp, bf16x8 q) {
  f32x16 o = {};
  float m = -3.0e38f;
  for (int t = 0; t < 64; ++t) {
    const bf16x8 kf = lds128(kp + t * 1024), v0 = lds128(vp + t * VT_BYTES), v1 = lds128(vp + t * VT_BYTES + 544);
    f32x16 s = MFMA(kf, q, f32x16{});
    const float mn = fmaxf(m, xhalf_max(max16(s)));
    const float al = __builtin_amdgcn_exp2f(m - mn);
    m = mn;
#pragma unroll
    for (int i = 0; i < 16; ++i) { o[i] *= al; s[i] = __builtin_amdgcn_exp2f(s[i] - mn); }
    o = MFMA(v0, pack8(s, 0), o);
    o = MFMA(v1, pack8(s, 8), o);
  }
  return o;
}
DI bool bad16(const f32x16& o) {
  bool bad = false;
#pragma unroll
  for (int i = 0; i < 9; ++i) bad |= !(fabsf(o[i]) <= 3.0e38f);
  return bad;
}
DI void attn_store(char* dst, const f32x16& o) {
  const float rl = 1.f / o[8];
  *(u32x4*)dst = u32x4{cvtpk(o[0] * rl, o[1] * rl), cvtpk(o[2] * rl, o[3] * rl), cvtpk(o[4] * rl, o[5] * rl), cvtpk(o[6] * rl, o[7] * rl)};
}

DI void attn_wave(const LAS char* lds, char* __restrict__ ws, int head, int qt0, int lane) {
  const int r = lane & 31, h = lane >> 5;
  const LAS char* kp = lds + KV_K + lane * 16;
  const LAS char* vp = lds + KV_V + h * 272 + (r < 16 ? r : 16) * 16;
  const bf16x8 qa = *(const bf16x8*)(ws + QF_OFF + ((size_t)head * 64 + qt0) * 1024 + lane * 16);
  const bf16x8 qb = *(const bf16x8*)(ws + QF_OFF + ((size_t)head * 64 + qt0 + 1) * 1024 + lane * 16);
  float ma = -3.0e38f, mb = -3.0e38f;
#pragma unroll
  for (int t = 0; t < 4; ++t) {
    const bf16x8 kf = lds128(kp + t * 1024);
    ma = fmaxf(ma, max16(MFMA(kf, qa, f32x16{})));
    mb = fmaxf(mb, max16(MFMA(kf, qb, f32x16{})));
  }
  ma = -(xhalf_max(ma) + M_OFFSET); mb = -(xhalf_max(mb) + M_OFFSET);
  f32x16 ca, cb, oa = {}, ob = {};
#pragma unroll
  for (int i = 0; i < 16; ++i) { ca[i] = ma; cb[i] = mb; }
  asm volatile("" : "+v"(ca), "+v"(cb));
  bf16x8 kf = lds128(kp);
  f32x16 sa = MFMA(kf, qa, ca), sb = MFMA(kf, qb, cb);
  for (int t = 0; t < 64; ++t) {
    const int tn = t < 63 ? t + 1 : 63;
    const bf16x8 v0 = lds128(vp + t * VT_BYTES), v1 = lds128(vp + t * VT_BYTES + 544);
    kf = lds128(kp + tn * 1024);
#pragma unroll
    for (int i = 0; i < 16; ++i) sa[i] = __builtin_amdgcn_exp2f(sa[i]);
    const bf16x8 pa0 = pack8(sa, 0), pa1 = pack8(sa, 8);
    oa = MFMA(v0, pa0, oa);
    oa = MFMA(v1, pa1, oa);
    sa = MFMA(kf, qa, ca);
#pragma unroll
    for (int i = 0; i < 16; ++i) sb[i] = __builtin_amdgcn_exp2f(sb[i]);
    const bf16x8 pb0 = pack8(sb, 0), pb1 = pack8(sb, 8);
    ob = MFMA(v0, pb0, ob);
    ob = MFMA(v1, pb1, ob);
    sb = MFMA(kf, qb, cb);
  }
  if (__any(bad16(oa))) oa = attn_slow(kp, vp, qa);
  if (__any(bad16(ob))) ob = attn_slow(kp, vp, qb);
  attn_store(ws + OF_OFF + ((size_t)qt0 * 32 + head) * 1024 + lane * 16, oa);
  attn_store(ws + OF_OFF + ((size_t)(qt0 + 1) * 32 + head) * 1024 + lane * 16, ob);
}

__global__ __launch_bounds__(256, 2) void k2_attn(char* __restrict__ ws) {
  extern __shared__ __attribute__((aligned(16))) char smem[];
  LAS char* lds = (LAS char*)smem;
  const unsigned lds0 = (unsigned)(size_t)lds;
  const int tid = threadIdx.x, lane = tid & 63, w = __builtin_amdgcn_readfirstlane(tid >> 6);
  const int b = blockIdx.x, xcd = b & 7, idx = b >> 3;
  const int head = xcd * 4 + (idx >> 3), qb = idx & 7;
  const char* ksrc = ws + KF_OFF + (size_t)head * 65536;
  const char* vsrc = ws + VF_OFF + (size_t)head * (64 * VT_BYTES);
  for (int n = w; n < 64; n += 4) glds16(lane * 16, ksrc + n * 1024, lds0 + KV_K + n * 1024);
  for (int n = w; n < 68; n += 4) glds16(lane * 16, vsrc + n * 1024, lds0 + KV_V + n * 1024);
  WAITBAR(0);
  attn_wave(lds, ws, head, qb * 8 + 2 * w, lane);
}

extern "C" void kernel_launch(void* const* d_in, const int* in_sizes, int n_in, void* d_out, int out_size, void* d_ws, size_t ws_size, hipStream_t stream) {
  const char* x = (const char*)d_in[0];
  const char* wqkv = (const char*)d_in[1];
  const char* wo = (const char*)d_in[2];
  const char* w1 = (const char*)d_in[4];
  const char* w2 = (const char*)d_in[5];
  char* ws = (char*)d_ws;
  hipFuncSetAttribute((const void*)k1_qkv, hipFuncAttributeMaxDynamicSharedMemorySize, K1_LDS);
  hipFuncSetAttribute((const void*)k2_attn, hipFuncAttributeMaxDynamicSharedMemorySize, KV_BYTES);
  hipFuncSetAttribute((const void*)k_ffn2, hipFuncAttributeMaxDynamicSharedMemorySize, 4 * 17408);
  hipFuncSetAttribute((const void*)k3_out, hipFuncAttributeMaxDynamicSharedMemorySize, 4 * 17408);
  k1_qkv<<<256, 512, K1_LDS, stream>>>(x, wqkv, wo, w1, w2, ws);
  k_ffn1<<<256, 256, 0, stream>>>(ws);
  k_ffn2<<<256, 256, 4 * 17408, stream>>>(ws);
  k2_attn<<<256, 256, KV_BYTES, stream>>>(ws);
  k3_out<<<256, 256, 4 * 17408, stream>>>(x, ws, (char*)d_out);
}
```

```cpp
#include <hip/hip_runtime.h>
#include <hip/hip_bf16.h>
#include <stdint.h>

typedef short bf16x8 __attribute__((ext_vector_type(8)));
typedef short s16x4 __attribute__((ext_vector_type(4)));
typedef float f32x16 __attribute__((ext_vector_type(16)));
typedef float f32x4 __attribute__((ext_vector_type(4)));
typedef float f32x2 __attribute__((ext_vector_type(2)));
typedef unsigned u32x4 __attribute__((ext_vector_type(4)));
typedef unsigned u32x2 __attribute__((ext_vector_type(2)));
#define LAS __attribute__((address_space(3)))
#define DI __device__ __forceinline__
#define MFMA(a, b, c) __builtin_amdgcn_mfma_f32_32x32x16_bf16((a), (b), (c), 0, 0, 0)
#define SBAR() __builtin_amdgcn_sched_barrier(0)

constexpr int SEQ = 2048, DM = 512, NH = 32, DFF = 1792;
constexpr int VT_BYTES = 1088;
constexpr size_t QF_OFF = 0;
constexpr size_t KF_OFF = QF_OFF + (size_t)NH * 64 * 1024;
constexpr size_t VF_OFF = KF_OFF + (size_t)NH * 64 * 1024;
constexpr size_t XN_OFF = VF_OFF + (size_t)NH * 64 * VT_BYTES;
constexpr size_t W1F_OFF = XN_OFF + (size_t)64 * 32 * 1024;
constexpr size_t W2F_OFF = W1F_OFF + (size_t)56 * 32 * 1024;
constexpr size_t WOF_OFF = W2F_OFF + (size_t)16 * 112 * 1024;
constexpr size_t HF_OFF = WOF_OFF + (size_t)16 * 32 * 1024;
constexpr size_t OF_OFF = HF_OFF + (size_t)64 * 112 * 1024;
constexpr size_t CNT_OFF = OF_OFF + (size_t)64 * 32 * 1024;
constexpr size_t WS_END = CNT_OFF + 8192;

DI unsigned cvtpk(float lo, float hi) {
  typedef __bf16 bf2 __attribute__((ext_vector_type(2)));
  f32x2 v = {lo, hi};
  bf2 r = __builtin_convertvector(v, bf2);
  return __builtin_bit_cast(unsigned, r);
}
DI float bf_lo(unsigned p) { return __uint_as_float(p << 16); }
DI float bf_hi(unsigned p) { return __uint_as_float(p & 0xffff0000u); }
DI float rbf(float x) { return bf_lo(cvtpk(x, 0.f)); }

DI void glds16(unsigned voff, const void* sbase, unsigned ldsbase) {
  unsigned sv;
  asm volatile("s_mov_b32 %0, m0\n\ts_mov_b32 m0, %3\n\ts_nop 0\n\tglobal_load_lds_dwordx4 %1, %2\n\ts_mov_b32 m0, %0"
               : "=&s"(sv) : "v"(voff), "s"(sbase), "s"(ldsbase) : "memory");
}
typedef short v4i16_t __attribute__((ext_vector_type(4)));
DI s16x4 trrd(const LAS char* p) { return __builtin_bit_cast(s16x4, __builtin_amdgcn_ds_read_tr16_b64_v4i16((LAS v4i16_t*)p)); }
DI bf16x8 cat(s16x4 lo, s16x4 hi) { return __builtin_shufflevector(lo, hi, 0, 1, 2, 3, 4, 5, 6, 7); }
DI bf16x8 lds128(const LAS char* p) { return *(const LAS bf16x8*)p; }

template <class T> DI void gst(void* p, T v) {
  asm volatile("global_store_dwordx4 %0, %1, off sc0 sc1\n\ts_nop 1" :: "v"(p), "v"(v) : "memory");
}
constexpr int K2_CODE_KB = 20;
DI unsigned code_prefetch(int piece, int lane) {
  unsigned t = 0;
  asm volatile("s_getpc_b64 s[90:91]\n\ts_add_u32 s90, s90, _Z11k2_attn_ffnPKcPcS1_@rel32@lo+4\n\ts_addc_u32 s91, s91, _Z11k2_attn_ffnPKcPcS1_@rel32@hi+12\n\t"
               "global_load_dword %0, %1, s[90:91]" : "=v"(t) : "v"((unsigned)(piece * 1024 + (lane & 7) * 128)) : "scc", "s90", "s91", "memory");
  return t;
}
constexpr int K1_SLOT = 32768, K1_SCR = K1_SLOT * 4;
constexpr int K1_ST = K1_SCR + 4 * 4096;
constexpr int K1_CS = K1_ST + 1024;
constexpr int K1_FIN = K1_CS + 2048;
constexpr int K1_FLG = K1_FIN + 1024;
constexpr int K1_LDS = K1_FLG + 64;
#define MFMA16(a, b, c) __builtin_amdgcn_mfma_f32_16x16x32_bf16((a), (b), (c), 0, 0, 0)
struct K1Frag { bf16x8 a[4]; s16x4 q[4][2], k[4][2], v[4][2]; };
#define BARL() asm volatile("s_waitcnt lgkmcnt(0)\n\ts_barrier" ::: "memory")
#define WAITV(N) asm volatile("s_waitcnt vmcnt(" #N ")" ::: "memory")
DI float xhalf_sum(float v) {
  auto rr = __builtin_amdgcn_permlane32_swap(__float_as_uint(v), __float_as_uint(v), false, false);
  return __uint_as_float(rr[0]) + __uint_as_float(rr[1]);
}
DI f32x2 ln_stats(float tx, float txx) {
  const float mean = tx * (1.f / DM);
  const float var = fmaxf(txx * (1.f / DM) - mean * mean, 0.f);
  return f32x2{rbf(mean), __builtin_amdgcn_rcpf(rbf(__builtin_amdgcn_sqrtf(rbf(rbf(var) + 1e-5f))))};
}
DI void lds_signal(LAS unsigned* p, int lane) {
  (void)lane;
  asm volatile("s_waitcnt lgkmcnt(0)\n\ts_mov_b64 exec, 1\n\tds_add_u32 %0, %1\n\ts_mov_b64 exec, -1" :: "v"((unsigned)(size_t)p), "v"(1u) : "memory");
}
template <int SL = 1>
DI void lds_wait(LAS unsigned* p, unsigned n) {
  while (__builtin_amdgcn_readfirstlane(*(volatile LAS unsigned*)p) < n) __builtin_amdgcn_s_sleep(SL);
  asm volatile("" ::: "memory");
}

__global__ __launch_bounds__(512, 2) void k1_qkv(const char* __restrict__ x, const char* __restrict__ wqkv, const char* __restrict__ wo,
                                                 const char* __restrict__ w1, const char* __restrict__ w2, char* __restrict__ ws) {
  extern __shared__ __attribute__((aligned(16))) char smem[];
  LAS char* lds = (LAS char*)smem;
  const unsigned lds0 = (unsigned)(size_t)lds;
  const int tid = threadIdx.x, lane = tid & 63, h = lane >> 5, r = lane & 31;
  const int w = __builtin_amdgcn_readfirstlane(tid >> 6), u = w & 3;
  const int b = blockIdx.x, xcd = b & 7, idx = b >> 3;
  const int rp = (xcd >> 1) * 8 + (idx >> 2), p = (xcd & 1) * 4 + (idx & 3);

  LAS unsigned* rdy = (LAS unsigned*)(lds + K1_FLG);
  LAS unsigned* dn = rdy + 8;
  if (tid < 16) rdy[tid] = 0;
  const unsigned va0 = (lane >> 3) * 1024 + (((lane & 7) ^ (lane >> 4)) << 4), va1 = va0 ^ 64;
  const unsigned vb = (lane >> 3) * 3072 + (((lane & 7) ^ ((lane >> 2) & 4)) << 4);
  const char* xa = x + (size_t)(rp * 64 + u * 16) * 1024;
  const char* wb = wqkv + p * 128;
  const int c0 = (xcd >> 1) * 2;
  unsigned vo1 = va1 + 8192, vbp[6];
#pragma unroll
  for (int e = 0; e < 6; ++e) { const int n = 6 * u + e; vbp[e] = vb + (n >> 3) * 1024 + (n & 7) * 24576; }
  auto issue = [&](int c) {
    const unsigned d = lds0 + (c & 3) * K1_SLOT;
    unsigned sv;
    asm volatile("s_mov_b32 %0, m0\n\ts_mov_b32 m0, %11\n\ts_nop 0\n\tglobal_load_lds_dwordx4 %1, %9\n\t"
                 "s_add_u32 m0, m0, 1024\n\ts_nop 0\n\tglobal_load_lds_dwordx4 %2, %9\n\t"
                 "s_mov_b32 m0, %12\n\ts_nop 0\n\tglobal_load_lds_dwordx4 %3, %10\n\t"
                 "s_add_u32 m0, m0, 1024\n\ts_nop 0\n\tglobal_load_lds_dwordx4 %4, %10\n\t"
                 "s_add_u32 m0, m0, 1024\n\ts_nop 0\n\tglobal_load_lds_dwordx4 %5, %10\n\t"
                 "s_add_u32 m0, m0, 1024\n\ts_nop 0\n\tglobal_load_lds_dwordx4 %6, %10\n\t"
                 "s_add_u32 m0, m0, 1024\n\ts_nop 0\n\tglobal_load_lds_dwordx4 %7, %10\n\t"
                 "s_add_u32 m0, m0, 1024\n\ts_nop 0\n\tglobal_load_lds_dwordx4 %8, %10\n\ts_mov_b32 m0, %0"
                 : "=&s"(sv) : "v"(va0), "v"(vo1), "v"(vbp[0]), "v"(vbp[1]), "v"(vbp[2]), "v"(vbp[3]), "v"(vbp[4]), "v"(vbp[5]),
                   "s"(xa + ((c + c0) & 7) * 128), "s"(wb + (size_t)((c + c0) & 7) * (64 * 3072)), "s"(d + u * 2048), "s"(d + 8192 + u * 6144) : "scc", "memory");
  };
  if (w < 4) issue(0);
  BARL();
  if (w < 4) issue(1); else { issue(2); issue(3); }

  const int i16 = lane & 15, q4 = i16 >> 2, p4 = i16 & 3, nh = (lane >> 4) & 1;
  LAS f32x2* stp = (LAS f32x2*)(lds + K1_ST);

  if (w >= 4) {
    __builtin_amdgcn_s_setprio(2);
    const char* rsrc[2]; char* rdst[2]; int rpitch, rperm, rdstep;
    {
      const int job = b * 8 + 2 * u;
      if (job < 896) { rpitch = DFF * 2; rperm = 0; rdstep = 32 * 1024;
#pragma unroll
        for (int e = 0; e < 2; ++e) { const int cp = (job + e) >> 5, ks = (job + e) & 31; rsrc[e] = w1 + (size_t)ks * 16 * (DFF * 2) + cp * 128; rdst[e] = ws + W1F_OFF + ((size_t)(2 * cp) * 32 + ks) * 1024; } }
      else if (job < 1792) { rpitch = DM * 2; rperm = 1; rdstep = 112 * 1024;
#pragma unroll
        for (int e = 0; e < 2; ++e) { const int jj = job + e - 896, cp = jj / 112, ks = jj - cp * 112; rsrc[e] = w2 + (size_t)ks * 16 * (DM * 2) + cp * 128; rdst[e] = ws + W2F_OFF + ((size_t)(2 * cp) * 112 + ks) * 1024; } }
      else { rpitch = DM * 2; rperm = 1; rdstep = 32 * 1024;
#pragma unroll
        for (int e = 0; e < 2; ++e) { const int jj = job + e - 1792, cp = jj >> 5, ks = jj & 31; rsrc[e] = wo + (size_t)ks * 16 * (DM * 2) + cp * 128; rdst[e] = ws + WOF_OFF + ((size_t)(2 * cp) * 32 + ks) * 1024; } }
    }
    u32x4 xf0, xf1;
    const int xrt = rp * 2 + (u & 1), xks = 4 * p + 2 * (u >> 1);
    auto extras = [&]() {
      const unsigned vo = (lane >> 3) * rpitch + (lane & 7) * 16;
      const unsigned d = lds0 + K1_SCR + u * 4096;
      glds16(vo, rsrc[0], d); glds16(vo, rsrc[0] + 8 * (size_t)rpitch, d + 1024);
      glds16(vo, rsrc[1], d + 2048); glds16(vo, rsrc[1] + 8 * (size_t)rpitch, d + 3072);
      const char* xp = x + (size_t)(xrt * 32 + r) * 1024 + xks * 32 + h * 16;
      asm volatile("global_load_dwordx4 %0, %2, off\n\tglobal_load_dwordx4 %1, %2, off offset:32" : "=&v"(xf0), "=&v"(xf1) : "v"(xp) : "memory");
    };
    lds_wait(dn + 0, 4); issue(4); WAITV(16); lds_signal(rdy + 2, lane);
    lds_wait(dn + 1, 4); issue(5); WAITV(16); lds_signal(rdy + 3, lane);
    lds_wait(dn + 2, 4); issue(6); WAITV(16); lds_signal(rdy + 4, lane);
    lds_wait(dn + 3, 4); issue(7); WAITV(16); lds_signal(rdy + 5, lane);
    extras();
    WAITV(14); lds_signal(rdy + 6, lane);
    WAITV(6); lds_signal(rdy + 7, lane);
    asm volatile("s_waitcnt vmcnt(0)" : "+v"(xf0), "+v"(xf1) :: "memory");
    unsigned pf = code_prefetch((idx * 4 + u) % K2_CODE_KB, lane);
    {
      const LAS char* sp = lds + K1_SCR + u * 4096 + nh * 32 + p4 * 8;
      const int r0 = rperm ? (4 * h + q4) : (8 * h + q4), r1 = rperm ? (8 + 4 * h + q4) : (8 * h + 4 + q4);
      gst(rdst[0] + lane * 16, cat(trrd(sp + r0 * 128), trrd(sp + r1 * 128)));
      gst(rdst[0] + (size_t)rdstep + lane * 16, cat(trrd(sp + r0 * 128 + 64), trrd(sp + r1 * 128 + 64)));
      gst(rdst[1] + lane * 16, cat(trrd(sp + 2048 + r0 * 128), trrd(sp + 2048 + r1 * 128)));
      gst(rdst[1] + (size_t)rdstep + lane * 16, cat(trrd(sp + 2048 + r0 * 128 + 64), trrd(sp + 2048 + r1 * 128 + 64)));
    }
    if (b == 0 && u == 0 && lane < 40) *(unsigned*)(ws + CNT_OFF + lane * 128) = 0;
    if (lane < 8) gst(ws + VF_OFF + ((size_t)(4 * p + u) * 64 + rp * 2 + (lane >> 2)) * VT_BYTES + (lane & 3) * 272 + 256, u32x4{0x3f803f80u, 0x3f803f80u, 0x3f803f80u, 0x3f803f80u});

    lds_wait<2>(dn + 4, 4);

    const f32x2 s0 = stp[(u & 1) * 32 + r], s1 = stp[((u & 1) + 2) * 32 + r];
    const f32x2 st = ln_stats(s0[0] + s1[0], s0[1] + s1[1]);
#pragma unroll
    for (int e = 0; e < 2; ++e) {
      const u32x4 xf = e ? xf1 : xf0;
      u32x4 o;
#pragma unroll
      for (int t = 0; t < 4; ++t) {
        const unsigned d = cvtpk(bf_lo(xf[t]) - st[0], bf_hi(xf[t]) - st[0]);
        o[t] = cvtpk(bf_lo(d) * st[1], bf_hi(d) * st[1]);
      }
      gst(ws + XN_OFF + ((size_t)xrt * 32 + xks + e) * 1024 + lane * 16, o);
    }
    asm volatile("s_waitcnt vmcnt(0)" : "+v"(pf) :: "memory");

    return;
  }

  const int m = w & 1, n = w >> 1, rt = rp * 2 + m;
  const int aoff = (32 * m + r) * 128 + ((h ^ ((r >> 1) & 7)) << 4);
  const int trl = 8192 + (8 * h + q4) * 128 + ((n ^ (q4 >> 1)) << 6) + nh * 32 + p4 * 8;
  int ao[4], bo[4];
#pragma unroll
  for (int e = 0; e < 4; ++e) { ao[e] = aoff ^ (((e + w) & 3) * 32); bo[e] = trl + ((e + w) & 3) * 2048; }
  f32x16 accq = {}, acck = {}, accv = {};
  f32x4 sx = {}, sq0 = {}, sq1 = {}, cq4 = {}, ck4 = {}, cv4 = {};
  bf16x8 sel;
  {
    const short sv = ((lane ^ (lane >> 4)) & 1) ? 0 : 0x3f80;
#pragma unroll
    for (int e = 0; e < 8; ++e) sel[e] = sv;
    asm volatile("" : "+v"(sel));
  }
  auto rd_all = [&](K1Frag& f, int slotoff) {
    const LAS char* sl = lds + slotoff;
#pragma unroll
    for (int e = 0; e < 4; ++e) {
      const LAS char* bp = sl + bo[e];
      f.a[e] = lds128(sl + ao[e]);
      f.q[e][0] = trrd(bp); f.q[e][1] = trrd(bp + 512);
      f.k[e][0] = trrd(bp + 8192); f.k[e][1] = trrd(bp + 8192 + 512);
      f.v[e][0] = trrd(bp + 16384); f.v[e][1] = trrd(bp + 16384 + 512);
    }
  };
  auto step = [&](const K1Frag& f, K1Frag& nx, int slotoff, bool rd) {
    const LAS char* sl = lds + slotoff;
#pragma unroll
    for (int e = 0; e < 4; ++e) {
      const LAS char* bp = sl + bo[e];
      const bf16x8 fq = cat(f.q[e][0], f.q[e][1]), fk = cat(f.k[e][0], f.k[e][1]), fv = cat(f.v[e][0], f.v[e][1]);
      const s16x4 alo = __builtin_shufflevector(f.a[e], f.a[e], 0, 1, 2, 3), ahi = __builtin_shufflevector(f.a[e], f.a[e], 4, 5, 6, 7);
      if (!(e & 1)) cq4 = MFMA16(sel, fq, cq4);
      if (e < 2) sx = MFMA16(sel, f.a[e], sx);
      SBAR();
      accq = MFMA(fq, f.a[e], accq);
      SBAR();
      if (rd) { nx.a[e] = lds128(sl + ao[e]); nx.q[e][0] = trrd(bp); nx.q[e][1] = trrd(bp + 512); }
      SBAR();
      if (!(e & 1)) ck4 = MFMA16(sel, fk, ck4);
      SBAR();
      acck = MFMA(fk, f.a[e], acck);
      SBAR();
      if (rd) { nx.k[e][0] = trrd(bp + 8192); nx.k[e][1] = trrd(bp + 8192 + 512); }
      SBAR();
      if (!(e & 1)) cv4 = MFMA16(sel, fv, cv4);
      if (e < 2) { sq0 = __builtin_amdgcn_mfma_f32_4x4x4bf16_1k(alo, alo, sq0, 0, 0, 0); sq1 = __builtin_amdgcn_mfma_f32_4x4x4bf16_1k(ahi, ahi, sq1, 0, 0, 0); }
      SBAR();
      accv = MFMA(f.a[e], fv, accv);
      SBAR();
      if (rd) { nx.v[e][0] = trrd(bp + 16384); nx.v[e][1] = trrd(bp + 16384 + 512); }
      SBAR();
    }
  };
  K1Frag f0, f1;
  WAITV(8); lds_signal(rdy + 0, lane);
  lds_wait(rdy + 0, 4);

  rd_all(f0, 0);
  lds_signal(dn + 0, lane);
  WAITV(0); lds_signal(rdy + 1, lane);
  lds_wait(rdy + 1, 4);
#pragma unroll
  for (int c = 0; c < 8; ++c) {
    unsigned fl = 4;
    if (c < 6) fl = *(volatile LAS unsigned*)(rdy + c + 2);
    if (c & 1) step(f1, f0, ((c + 1) & 3) * K1_SLOT, c < 7); else step(f0, f1, ((c + 1) & 3) * K1_SLOT, true);
    if (c < 3) lds_signal(dn + 1 + c, lane);
    if (c < 6 && __builtin_expect(__builtin_amdgcn_readfirstlane(fl) < 4u, 0)) lds_wait(rdy + c + 2, 4);
  }

  float tx, txx;
  {
    const f32x4 sq = sq0 + sq1;
    const float own = (lane & 2) ? ((lane & 1) ? sq[3] : sq[2]) : ((lane & 1) ? sq[1] : sq[0]);
    tx = nh ? sx[1] : sx[0]; txx = xhalf_sum(own);
  }
  float cq = nh ? cq4[1] : cq4[0], ck = nh ? ck4[1] : ck4[0], cv = nh ? cv4[1] : cv4[0];
  LAS f32x4* csp = (LAS f32x4*)(lds + K1_CS);
  if (h == 0) { stp[w * 32 + r] = f32x2{tx, txx}; csp[w * 32 + r] = f32x4{cq, ck, cv, 0.f}; }
  lds_signal(dn + 4, lane);
  lds_wait<0>(dn + 4, 4);

  {
    const f32x2 o2 = stp[(w ^ 2) * 32 + r];
    const f32x4 c2 = csp[(w ^ 1) * 32 + r];
    tx += o2[0]; txx += o2[1]; cq += c2[0]; ck += c2[1]; cv += c2[2];
  }
  const f32x2 st = ln_stats(tx, txx);
  const float rinv = st[1], rq = rinv * (0.25f * 1.4426950408889634f);
  auto split3 = [&](float v) {
    const float hi = rbf(v), mi = rbf(v - hi);
    const u32x4 t = {h ? 0u : cvtpk(hi, mi), h ? 0u : cvtpk(v - hi - mi, 0.f), 0u, 0u};
    return __builtin_bit_cast(bf16x8, t);
  };
  const unsigned nm = cvtpk(-st[0], -st[0]);
  const bf16x8 fm = __builtin_bit_cast(bf16x8, u32x4{nm, nm, 0u, 0u}), fone = __builtin_bit_cast(bf16x8, u32x4{0x3f803f80u, 0x3f803f80u, 0u, 0u});
  accq = MFMA(split3(cq), fm, accq);
  acck = MFMA(split3(ck), fm, acck);
  accv = MFMA(fm, split3(cv), accv);
  const f32x16 rv = MFMA(split3(rinv), fone, f32x16{});
  const int hd = 4 * p + 2 * n;
#pragma unroll
  for (int s2 = 0; s2 < 2; ++s2) {
    u32x4 oq, ok;
#pragma unroll
    for (int t = 0; t < 4; ++t) {
      oq[t] = cvtpk(accq[8 * s2 + 2 * t] * rq, accq[8 * s2 + 2 * t + 1] * rq);
      ok[t] = cvtpk(acck[8 * s2 + 2 * t] * rinv, acck[8 * s2 + 2 * t + 1] * rinv);
    }
    const size_t off = ((size_t)(hd + s2) * 64 + rt) * 1024 + lane * 16;
    gst(ws + QF_OFF + off, oq);
    gst(ws + KF_OFF + off, ok);
  }
  char* vb2 = ws + VF_OFF + ((size_t)(hd + (r >> 4)) * 64 + rt) * VT_BYTES + h * 272;
#pragma unroll
  for (int s2 = 0; s2 < 2; ++s2) {
    u32x4 ov;
#pragma unroll
    for (int t = 0; t < 4; ++t) ov[t] = cvtpk(accv[8 * s2 + 2 * t] * rv[8 * s2 + 2 * t], accv[8 * s2 + 2 * t + 1] * rv[8 * s2 + 2 * t + 1]);
    gst(vb2 + s2 * 544 + (r & 15) * 16, ov);
  }

}

#define GPRIO(x) __builtin_amdgcn_s_setprio(x)
typedef __amdgpu_buffer_rsrc_t rsrc_t;
DI rsrc_t mkrsrc(const void* p, unsigned bytes) { return __builtin_amdgcn_make_buffer_rsrc((void*)p, 0, bytes, 0x00020000); }
template <int AUX> DI bf16x8 bld(rsrc_t r, unsigned voff, unsigned soff) { return __builtin_bit_cast(bf16x8, __builtin_amdgcn_raw_buffer_load_b128(r, voff, soff, AUX)); }

template <int NC, int NK, int PF, int AUXA, bool LOWPRIO = true>
DI void gemm_frag(rsrc_t ra, unsigned aoff, unsigned astride, rsrc_t rb, unsigned boff, unsigned bstride, unsigned voff, f32x16 (&acc)[2][NC]) {
  constexpr int R = PF + 1, NMAIN = (NK - R - PF) / R * R + R;
  bf16x8 a[R][2], bb[R][NC];
  auto ld = [&](int slot, unsigned k) {
    a[slot][0] = bld<AUXA>(ra, voff, aoff + k * 1024); a[slot][1] = bld<AUXA>(ra, voff, aoff + astride + k * 1024);
#pragma unroll
    for (int j = 0; j < NC; ++j) bb[slot][j] = bld<0>(rb, voff, boff + j * bstride + k * 1024);
  };
  auto mm = [&](int slot) {
#pragma unroll
    for (int i = 0; i < 2; ++i)
#pragma unroll
      for (int j = 0; j < NC; ++j) acc[i][j] = MFMA(bb[slot][j], a[slot][i], acc[i][j]);
  };
#pragma unroll
  for (int k = 0; k < PF; ++k) ld(k, k);
  if (LOWPRIO) GPRIO(0);
  SBAR();
  ld(PF, PF);
#pragma unroll
  for (int i = 0; i < 2; ++i)
#pragma unroll
    for (int j = 0; j < NC; ++j) acc[i][j] = MFMA(bb[0][j], a[0][i], f32x16{});
  SBAR();
#pragma unroll
  for (int kk = 1; kk < R; ++kk) { ld((kk + PF) % R, kk + PF); mm(kk); SBAR(); }
#pragma unroll 1
  for (int k0 = R; k0 < NMAIN; k0 += R) {
#pragma unroll
    for (int kk = 0; kk < R; ++kk) { ld((kk + PF) % R, k0 + kk + PF); mm(kk); SBAR(); }
  }
#pragma unroll
  for (int k = NMAIN; k < NK; ++k) { if (k + PF < NK) ld((k + PF) % R, k + PF); mm(k % R); SBAR(); }
  if (LOWPRIO) GPRIO(1);
}

template <int NC>
DI void ffn1_wave(char* __restrict__ ws, int rt0, int ct0, int lane) {
  f32x16 acc[2][NC];
  gemm_frag<NC, 32, 7, 0>(mkrsrc(ws + XN_OFF, 64 * 32 * 1024), rt0 * 32 * 1024, 32 * 1024, mkrsrc(ws + W1F_OFF, 56 * 32 * 1024), ct0 * 32 * 1024, 32 * 1024, lane * 16, acc);
  const rsrc_t hr = mkrsrc(ws + HF_OFF, 64 * 112 * 1024);
#pragma unroll
  for (int i = 0; i < 2; ++i)
#pragma unroll
    for (int j = 0; j < NC; ++j)
#pragma unroll
      for (int s = 0; s < 2; ++s) {
        u32x4 o;
#pragma unroll
        for (int t = 0; t < 4; ++t) {
          const unsigned pk = cvtpk(acc[i][j][8 * s + 2 * t], acc[i][j][8 * s + 2 * t + 1]);
          asm("v_pk_max_i16 %0, %1, 0" : "=v"(o[t]) : "v"(pk));
        }
        __builtin_amdgcn_raw_buffer_store_b128(o, hr, lane * 16 + ((rt0 + i) * 112 + 2 * (ct0 + j) + s) * 1024, 0, 16);
      }
}
DI void dump22(LAS char* img, const f32x16 (&acc)[2][2], int r, int h) {
#pragma unroll
  for (int i = 0; i < 2; ++i)
#pragma unroll
    for (int j = 0; j < 2; ++j)
#pragma unroll
      for (int g = 0; g < 4; ++g)
        *(LAS f32x4*)(img + (32 * i + r) * 272 + (32 * j + 8 * g + 4 * h) * 4) = f32x4{acc[i][j][4 * g], acc[i][j][4 * g + 1], acc[i][j][4 * g + 2], acc[i][j][4 * g + 3]};
}

constexpr int KV_K = 0, KV_V = 65536, KV_BYTES = 65536 + 65 * VT_BYTES;
constexpr float M_OFFSET = 100.f;

DI float max16(const f32x16& s) {
  float a = fmaxf(fmaxf(s[0], s[1]), s[2]), c = fmaxf(fmaxf(s[3], s[4]), s[5]);
  a = fmaxf(fmaxf(a, s[6]), s[7]); c = fmaxf(fmaxf(c, s[8]), s[9]);
  a = fmaxf(fmaxf(a, s[10]), s[11]); c = fmaxf(fmaxf(c, s[12]), s[13]);
  a = fmaxf(fmaxf(a, s[14]), s[15]);
  return fmaxf(a, c);
}
DI float xhalf_max(float m) {
  auto rr = __builtin_amdgcn_permlane32_swap(__float_as_uint(m), __float_as_uint(m), false, false);
  return fmaxf(__uint_as_float(rr[0]), __uint_as_float(rr[1]));
}
DI bf16x8 pack8(const f32x16& s, int o) {
  u32x4 p = {cvtpk(s[o], s[o + 1]), cvtpk(s[o + 2], s[o + 3]), cvtpk(s[o + 4], s[o + 5]), cvtpk(s[o + 6], s[o + 7])};
  return __builtin_bit_cast(bf16x8, p);
}
__device__ __noinline__ f32x16 attn_slow(const LAS char* kp, const LAS char* vp, bf16x8 q) {
  f32x16 o = {};
  float m = -3.0e38f;
  for (int t = 0; t < 64; ++t) {
    const bf16x8 kf = lds128(kp + t * 1024), v0 = lds128(vp + t * VT_BYTES), v1 = lds128(vp + t * VT_BYTES + 544);
    f32x16 s = MFMA(kf, q, f32x16{});
    const float mn = fmaxf(m, xhalf_max(max16(s)));
    const float al = __builtin_amdgcn_exp2f(m - mn);
    m = mn;
#pragma unroll
    for (int i = 0; i < 16; ++i) { o[i] *= al; s[i] = __builtin_amdgcn_exp2f(s[i] - mn); }
    o = MFMA(v0, pack8(s, 0), o);
    o = MFMA(v1, pack8(s, 8), o);
  }
  return o;
}
DI bool bad16(const f32x16& o) {
  bool bad = !(o[8] >= 1.0e-36f);
#pragma unroll
  for (int i = 0; i < 9; ++i) bad |= !(fabsf(o[i]) <= 3.0e38f);
  return bad;
}
DI void attn_store(rsrc_t ro, unsigned off, const f32x16& o) {
  const float rl = 1.f / o[8];
  __builtin_amdgcn_raw_buffer_store_b128(u32x4{cvtpk(o[0] * rl, o[1] * rl), cvtpk(o[2] * rl, o[3] * rl), cvtpk(o[4] * rl, o[5] * rl), cvtpk(o[6] * rl, o[7] * rl)}, ro, off, 0, 16);
}

struct AttnState { f32x16 oa, ob, sa, sb, ca, cb; bf16x8 qa, qb, pa0, pa1, pb0, pb1, k0, v00, v01, k1, v10, v11; unsigned kad, vad; };
DI void attn_loop(AttnState& s, int n, unsigned flagaddr) {
  unsigned fl, fv;
  asm volatile("s_nop 7\n\ts_nop 7\n\t"
"1:\n\t"
"v_exp_f32 v32, v32\n\t"
"v_exp_f32 v33, v33\n\t"
"v_mfma_f32_32x32x16_bf16 v[16:31], v[140:143], v[116:119], v[16:31]\n\t"
"ds_read_b128 v[132:135], v144 offset:1024\n\t"
"v_exp_f32 v34, v34\n\t"
"v_cvt_pk_bf16_f32 v104, v32, v33\n\t"
"v_exp_f32 v35, v35\n\t"
"v_exp_f32 v36, v36\n\t"
"v_cvt_pk_bf16_f32 v105, v34, v35\n\t"
"v_exp_f32 v37, v37\n\t"
"v_exp_f32 v38, v38\n\t"
"v_mfma_f32_32x32x16_bf16 v[48:63], v[120:123], v[100:103], v[80:95]\n\t"
"ds_read_b128 v[136:139], v145 offset:1088\n\t"
"v_cvt_pk_bf16_f32 v106, v36, v37\n\t"
"v_exp_f32 v39, v39\n\t"
"v_exp_f32 v40, v40\n\t"
"v_cvt_pk_bf16_f32 v107, v38, v39\n\t"
"v_exp_f32 v41, v41\n\t"
"v_exp_f32 v42, v42\n\t"
"v_mfma_f32_32x32x16_bf16 v[0:15], v[124:127], v[104:107], v[0:15]\n\t"
"ds_read_b128 v[140:143], v145 offset:1632\n\t"
"v_cvt_pk_bf16_f32 v108, v40, v41\n\t"
"v_exp_f32 v43, v43\n\t"
"v_exp_f32 v44, v44\n\t"
"v_cvt_pk_bf16_f32 v109, v42, v43\n\t"
"v_exp_f32 v45, v45\n\t"
"v_exp_f32 v46, v46\n\t"
"v_exp_f32 v47, v47\n\t"
"v_cvt_pk_bf16_f32 v110, v44, v45\n\t"
"v_cvt_pk_bf16_f32 v111, v46, v47\n\t"
"v_exp_f32 v48, v48\n\t"
"v_exp_f32 v49, v49\n\t"
"v_mfma_f32_32x32x16_bf16 v[0:15], v[128:131], v[108:111], v[0:15]\n\t"
"s_waitcnt lgkmcnt(0)\n\t"
"v_exp_f32 v50, v50\n\t"
"v_cvt_pk_bf16_f32 v112, v48, v49\n\t"
"v_exp_f32 v51, v51\n\t"
"v_exp_f32 v52, v52\n\t"
"v_cvt_pk_bf16_f32 v113, v50, v51\n\t"
"v_exp_f32 v53, v53\n\t"
"v_exp_f32 v54, v54\n\t"
"v_mfma_f32_32x32x16_bf16 v[32:47], v[132:135], v[96:99], v[64:79]\n\t"
"v_cvt_pk_bf16_f32 v114, v52, v53\n\t"
"v_exp_f32 v55, v55\n\t"
"v_exp_f32 v56, v56\n\t"
"v_cvt_pk_bf16_f32 v115, v54, v55\n\t"
"v_exp_f32 v57, v57\n\t"
"v_exp_f32 v58, v58\n\t"
"v_mfma_f32_32x32x16_bf16 v[16:31], v[124:127], v[112:115], v[16:31]\n\t"
"v_cvt_pk_bf16_f32 v116, v56, v57\n\t"
"v_exp_f32 v59, v59\n\t"
"v_exp_f32 v60, v60\n\t"
"v_cvt_pk_bf16_f32 v117, v58, v59\n\t"
"v_exp_f32 v61, v61\n\t"
"v_exp_f32 v62, v62\n\t"
"v_exp_f32 v63, v63\n\t"
"v_cvt_pk_bf16_f32 v118, v60, v61\n\t"
"v_cvt_pk_bf16_f32 v119, v62, v63\n\t"
"v_exp_f32 v32, v32\n\t"
"v_exp_f32 v33, v33\n\t"
"v_mfma_f32_32x32x16_bf16 v[16:31], v[128:131], v[116:119], v[16:31]\n\t"
"ds_read_b128 v[120:123], v144 offset:2048\n\t"
"v_exp_f32 v34, v34\n\t"
"v_cvt_pk_bf16_f32 v104, v32, v33\n\t"
"v_exp_f32 v35, v35\n\t"
"v_exp_f32 v36, v36\n\t"
"v_cvt_pk_bf16_f32 v105, v34, v35\n\t"
"v_exp_f32 v37, v37\n\t"
"v_exp_f32 v38, v38\n\t"
"v_mfma_f32_32x32x16_bf16 v[48:63], v[132:135], v[100:103], v[80:95]\n\t"
"ds_read_b128 v[124:127], v145 offset:2176\n\t"
"v_cvt_pk_bf16_f32 v106, v36, v37\n\t"
"v_exp_f32 v39, v39\n\t"
"v_exp_f32 v40, v40\n\t"
"v_cvt_pk_bf16_f32 v107, v38, v39\n\t"
"v_exp_f32 v41, v41\n\t"
"v_exp_f32 v42, v42\n\t"
"v_mfma_f32_32x32x16_bf16 v[0:15], v[136:139], v[104:107], v[0:15]\n\t"
"ds_read_b128 v[128:131], v145 offset:2720\n\t"
"v_cvt_pk_bf16_f32 v108, v40, v41\n\t"
"v_exp_f32 v43, v43\n\t"
"v_exp_f32 v44, v44\n\t"
"v_cvt_pk_bf16_f32 v109, v42, v43\n\t"
"v_exp_f32 v45, v45\n\t"
"v_exp_f32 v46, v46\n\t"
"v_exp_f32 v47, v47\n\t"
"v_cvt_pk_bf16_f32 v110, v44, v45\n\t"
"v_cvt_pk_bf16_f32 v111, v46, v47\n\t"
"v_exp_f32 v48, v48\n\t"
"v_exp_f32 v49, v49\n\t"
"v_mfma_f32_32x32x16_bf16 v[0:15], v[140:143], v[108:111], v[0:15]\n\t"
"s_waitcnt lgkmcnt(0)\n\t"
"v_exp_f32 v50, v50\n\t"
"v_cvt_pk_bf16_f32 v112, v48, v49\n\t"
"v_exp_f32 v51, v51\n\t"
"v_exp_f32 v52, v52\n\t"
"v_cvt_pk_bf16_f32 v113, v50, v51\n\t"
"v_exp_f32 v53, v53\n\t"
"v_exp_f32 v54, v54\n\t"
"v_mfma_f32_32x32x16_bf16 v[32:47], v[120:123], v[96:99], v[64:79]\n\t"
"v_cvt_pk_bf16_f32 v114, v52, v53\n\t"
"v_exp_f32 v55, v55\n\t"
"v_exp_f32 v56, v56\n\t"
"v_cvt_pk_bf16_f32 v115, v54, v55\n\t"
"v_exp_f32 v57, v57\n\t"
"v_exp_f32 v58, v58\n\t"
"v_mfma_f32_32x32x16_bf16 v[16:31], v[136:139], v[112:115], v[16:31]\n\t"
"v_cvt_pk_bf16_f32 v116, v56, v57\n\t"
"v_exp_f32 v59, v59\n\t"
"v_exp_f32 v60, v60\n\t"
"v_cvt_pk_bf16_f32 v117, v58, v59\n\t"
"v_exp_f32 v61, v61\n\t"
"v_exp_f32 v62, v62\n\t"
"v_exp_f32 v63, v63\n\t"
"v_cvt_pk_bf16_f32 v118, v60, v61\n\t"
"v_cvt_pk_bf16_f32 v119, v62, v63\n\t"
"v_exp_f32 v32, v32\n\t"
"v_exp_f32 v33, v33\n\t"
"v_mfma_f32_32x32x16_bf16 v[16:31], v[140:143], v[116:119], v[16:31]\n\t"
"ds_read_b128 v[132:135], v144 offset:3072\n\t"
"v_exp_f32 v34, v34\n\t"
"v_cvt_pk_bf16_f32 v104, v32, v33\n\t"
"v_exp_f32 v35, v35\n\t"
"v_exp_f32 v36, v36\n\t"
"v_cvt_pk_bf16_f32 v105, v34, v35\n\t"
"v_exp_f32 v37, v37\n\t"
"v_exp_f32 v38, v38\n\t"
"v_mfma_f32_32x32x16_bf16 v[48:63], v[120:123], v[100:103], v[80:95]\n\t"
"ds_read_b128 v[136:139], v145 offset:3264\n\t"
"v_cvt_pk_bf16_f32 v106, v36, v37\n\t"
"v_exp_f32 v39, v39\n\t"
"v_exp_f32 v40, v40\n\t"
"v_cvt_pk_bf16_f32 v107, v38, v39\n\t"
"v_exp_f32 v41, v41\n\t"
"v_exp_f32 v42, v42\n\t"
"v_mfma_f32_32x32x16_bf16 v[0:15], v[124:127], v[104:107], v[0:15]\n\t"
"ds_read_b128 v[140:143], v145 offset:3808\n\t"
"v_cvt_pk_bf16_f32 v108, v40, v41\n\t"
"v_exp_f32 v43, v43\n\t"
"v_exp_f32 v44, v44\n\t"
"v_cvt_pk_bf16_f32 v109, v42, v43\n\t"
"v_exp_f32 v45, v45\n\t"
"v_exp_f32 v46, v46\n\t"
"v_exp_f32 v47, v47\n\t"
"v_cvt_pk_bf16_f32 v110, v44, v45\n\t"
"v_cvt_pk_bf16_f32 v111, v46, v47\n\t"
"v_exp_f32 v48, v48\n\t"
"v_exp_f32 v49, v49\n\t"
"v_mfma_f32_32x32x16_bf16 v[0:15], v[128:131], v[108:111], v[0:15]\n\t"
"s_waitcnt lgkmcnt(0)\n\t"
"v_exp_f32 v50, v50\n\t"
"v_cvt_pk_bf16_f32 v112, v48, v49\n\t"
"v_exp_f32 v51, v51\n\t"
"v_exp_f32 v52, v52\n\t"
"v_cvt_pk_bf16_f32 v113, v50, v51\n\t"
"v_exp_f32 v53, v53\n\t"
"v_exp_f32 v54, v54\n\t"
"v_mfma_f32_32x32x16_bf16 v[32:47], v[132:135], v[96:99], v[64:79]\n\t"
"v_cvt_pk_bf16_f32 v114, v52, v53\n\t"
"v_exp_f32 v55, v55\n\t"
"v_exp_f32 v56, v56\n\t"
"v_cvt_pk_bf16_f32 v115, v54, v55\n\t"
"v_exp_f32 v57, v57\n\t"
"v_exp_f32 v58, v58\n\t"
"v_mfma_f32_32x32x16_bf16 v[16:31], v[124:127], v[112:115], v[16:31]\n\t"
"v_cvt_pk_bf16_f32 v116, v56, v57\n\t"
"v_exp_f32 v59, v59\n\t"
"v_exp_f32 v60, v60\n\t"
"v_cvt_pk_bf16_f32 v117, v58, v59\n\t"
"v_exp_f32 v61, v61\n\t"
"v_exp_f32 v62, v62\n\t"
"v_exp_f32 v63, v63\n\t"
"v_cvt_pk_bf16_f32 v118, v60, v61\n\t"
"v_cvt_pk_bf16_f32 v119, v62, v63\n\t"
"v_exp_f32 v32, v32\n\t"
"v_exp_f32 v33, v33\n\t"
"v_mfma_f32_32x32x16_bf16 v[16:31], v[128:131], v[116:119], v[16:31]\n\t"
"ds_read_b128 v[120:123], v144 offset:4096\n\t"
"v_exp_f32 v34, v34\n\t"
"v_cvt_pk_bf16_f32 v104, v32, v33\n\t"
"v_exp_f32 v35, v35\n\t"
"v_exp_f32 v36, v36\n\t"
"v_cvt_pk_bf16_f32 v105, v34, v35\n\t"
"v_exp_f32 v37, v37\n\t"
"v_exp_f32 v38, v38\n\t"
"v_mfma_f32_32x32x16_bf16 v[48:63], v[132:135], v[100:103], v[80:95]\n\t"
"ds_read_b128 v[124:127], v145 offset:4352\n\t"
"v_cvt_pk_bf16_f32 v106, v36, v37\n\t"
"v_exp_f32 v39, v39\n\t"
"v_exp_f32 v40, v40\n\t"
"v_cvt_pk_bf16_f32 v107, v38, v39\n\t"
"v_exp_f32 v41, v41\n\t"
"v_exp_f32 v42, v42\n\t"
"v_mfma_f32_32x32x16_bf16 v[0:15], v[136:139], v[104:107], v[0:15]\n\t"
"ds_read_b128 v[128:131], v145 offset:4896\n\t"
"v_cvt_pk_bf16_f32 v108, v40, v41\n\t"
"v_exp_f32 v43, v43\n\t"
"v_exp_f32 v44, v44\n\t"
"v_cvt_pk_bf16_f32 v109, v42, v43\n\t"
"v_exp_f32 v45, v45\n\t"
"v_exp_f32 v46, v46\n\t"
"v_exp_f32 v47, v47\n\t"
"v_cvt_pk_bf16_f32 v110, v44, v45\n\t"
"v_cvt_pk_bf16_f32 v111, v46, v47\n\t"
"v_exp_f32 v48, v48\n\t"
"v_exp_f32 v49, v49\n\t"
"v_mfma_f32_32x32x16_bf16 v[0:15], v[140:143], v[108:111], v[0:15]\n\t"
"s_waitcnt lgkmcnt(0)\n\t"
"v_exp_f32 v50, v50\n\t"
"v_cvt_pk_bf16_f32 v112, v48, v49\n\t"
"v_exp_f32 v51, v51\n\t"
"v_exp_f32 v52, v52\n\t"
"v_cvt_pk_bf16_f32 v113, v50, v51\n\t"
"v_exp_f32 v53, v53\n\t"
"v_exp_f32 v54, v54\n\t"
"v_mfma_f32_32x32x16_bf16 v[32:47], v[120:123], v[96:99], v[64:79]\n\t"
"v_cvt_pk_bf16_f32 v114, v52, v53\n\t"
"v_exp_f32 v55, v55\n\t"
"v_exp_f32 v56, v56\n\t"
"v_cvt_pk_bf16_f32 v115, v54, v55\n\t"
"v_exp_f32 v57, v57\n\t"
"v_exp_f32 v58, v58\n\t"
"v_mfma_f32_32x32x16_bf16 v[16:31], v[136:139], v[112:115], v[16:31]\n\t"
"v_cvt_pk_bf16_f32 v116, v56, v57\n\t"
"v_exp_f32 v59, v59\n\t"
"v_exp_f32 v60, v60\n\t"
"v_cvt_pk_bf16_f32 v117, v58, v59\n\t"
"v_exp_f32 v61, v61\n\t"
"v_exp_f32 v62, v62\n\t"
"v_exp_f32 v63, v63\n\t"
"v_cvt_pk_bf16_f32 v118, v60, v61\n\t"
"v_cvt_pk_bf16_f32 v119, v62, v63\n\t"
"v_exp_f32 v32, v32\n\t"
"v_exp_f32 v33, v33\n\t"
"v_mfma_f32_32x32x16_bf16 v[16:31], v[140:143], v[116:119], v[16:31]\n\t"
"ds_read_b128 v[132:135], v144 offset:5120\n\t"
"v_exp_f32 v34, v34\n\t"
"v_cvt_pk_bf16_f32 v104, v32, v33\n\t"
"v_exp_f32 v35, v35\n\t"
"v_exp_f32 v36, v36\n\t"
"v_cvt_pk_bf16_f32 v105, v34, v35\n\t"
"v_exp_f32 v37, v37\n\t"
"v_exp_f32 v38, v38\n\t"
"v_mfma_f32_32x32x16_bf16 v[48:63], v[120:123], v[100:103], v[80:95]\n\t"
"ds_read_b128 v[136:139], v145 offset:5440\n\t"
"v_cvt_pk_bf16_f32 v106, v36, v37\n\t"
"v_exp_f32 v39, v39\n\t"
"v_exp_f32 v40, v40\n\t"
"v_cvt_pk_bf16_f32 v107, v38, v39\n\t"
"v_exp_f32 v41, v41\n\t"
"v_exp_f32 v42, v42\n\t"
"v_mfma_f32_32x32x16_bf16 v[0:15], v[124:127], v[104:107], v[0:15]\n\t"
"ds_read_b128 v[140:143], v145 offset:5984\n\t"
"v_cvt_pk_bf16_f32 v108, v40, v41\n\t"
"v_exp_f32 v43, v43\n\t"
"v_exp_f32 v44, v44\n\t"
"v_cvt_pk_bf16_f32 v109, v42, v43\n\t"
"v_exp_f32 v45, v45\n\t"
"v_exp_f32 v46, v46\n\t"
"v_exp_f32 v47, v47\n\t"
"v_cvt_pk_bf16_f32 v110, v44, v45\n\t"
"v_cvt_pk_bf16_f32 v111, v46, v47\n\t"
"v_exp_f32 v48, v48\n\t"
"v_exp_f32 v49, v49\n\t"
"v_mfma_f32_32x32x16_bf16 v[0:15], v[128:131], v[108:111], v[0:15]\n\t"
"s_waitcnt lgkmcnt(0)\n\t"
"v_exp_f32 v50, v50\n\t"
"v_cvt_pk_bf16_f32 v112, v48, v49\n\t"
"v_exp_f32 v51, v51\n\t"
"v_exp_f32 v52, v52\n\t"
"v_cvt_pk_bf16_f32 v113, v50, v51\n\t"
"v_exp_f32 v53, v53\n\t"
"v_exp_f32 v54, v54\n\t"
"v_mfma_f32_32x32x16_bf16 v[32:47], v[132:135], v[96:99], v[64:79]\n\t"
"v_cvt_pk_bf16_f32 v114, v52, v53\n\t"
"v_exp_f32 v55, v55\n\t"
"v_exp_f32 v56, v56\n\t"
"v_cvt_pk_bf16_f32 v115, v54, v55\n\t"
"v_exp_f32 v57, v57\n\t"
"v_exp_f32 v58, v58\n\t"
"v_mfma_f32_32x32x16_bf16 v[16:31], v[124:127], v[112:115], v[16:31]\n\t"
"v_cvt_pk_bf16_f32 v116, v56, v57\n\t"
"v_exp_f32 v59, v59\n\t"
"v_exp_f32 v60, v60\n\t"
"v_cvt_pk_bf16_f32 v117, v58, v59\n\t"
"v_exp_f32 v61, v61\n\t"
"v_exp_f32 v62, v62\n\t"
"v_exp_f32 v63, v63\n\t"
"v_cvt_pk_bf16_f32 v118, v60, v61\n\t"
"v_cvt_pk_bf16_f32 v119, v62, v63\n\t"
"v_exp_f32 v32, v32\n\t"
"v_exp_f32 v33, v33\n\t"
"v_mfma_f32_32x32x16_bf16 v[16:31], v[128:131], v[116:119], v[16:31]\n\t"
"ds_read_b128 v[120:123], v144 offset:6144\n\t"
"v_exp_f32 v34, v34\n\t"
"v_cvt_pk_bf16_f32 v104, v32, v33\n\t"
"v_exp_f32 v35, v35\n\t"
"v_exp_f32 v36, v36\n\t"
"v_cvt_pk_bf16_f32 v105, v34, v35\n\t"
"v_exp_f32 v37, v37\n\t"
"v_exp_f32 v38, v38\n\t"
"v_mfma_f32_32x32x16_bf16 v[48:63], v[132:135], v[100:103], v[80:95]\n\t"
"ds_read_b128 v[124:127], v145 offset:6528\n\t"
"v_cvt_pk_bf16_f32 v106, v36, v37\n\t"
"v_exp_f32 v39, v39\n\t"
"v_exp_f32 v40, v40\n\t"
"v_cvt_pk_bf16_f32 v107, v38, v39\n\t"
"v_exp_f32 v41, v41\n\t"
"v_exp_f32 v42, v42\n\t"
"v_mfma_f32_32x32x16_bf16 v[0:15], v[136:139], v[104:107], v[0:15]\n\t"
"ds_read_b128 v[128:131], v145 offset:7072\n\t"
"v_cvt_pk_bf16_f32 v108, v40, v41\n\t"
"v_exp_f32 v43, v43\n\t"
"v_exp_f32 v44, v44\n\t"
"v_cvt_pk_bf16_f32 v109, v42, v43\n\t"
"v_exp_f32 v45, v45\n\t"
"v_exp_f32 v46, v46\n\t"
"v_exp_f32 v47, v47\n\t"
"v_cvt_pk_bf16_f32 v110, v44, v45\n\t"
"v_cvt_pk_bf16_f32 v111, v46, v47\n\t"
"v_exp_f32 v48, v48\n\t"
"v_exp_f32 v49, v49\n\t"
"v_mfma_f32_32x32x16_bf16 v[0:15], v[140:143], v[108:111], v[0:15]\n\t"
"s_waitcnt lgkmcnt(0)\n\t"
"v_exp_f32 v50, v50\n\t"
"v_cvt_pk_bf16_f32 v112, v48, v49\n\t"
"v_exp_f32 v51, v51\n\t"
"v_exp_f32 v52, v52\n\t"
"v_cvt_pk_bf16_f32 v113, v50, v51\n\t"
"v_exp_f32 v53, v53\n\t"
"v_exp_f32 v54, v54\n\t"
"v_mfma_f32_32x32x16_bf16 v[32:47], v[120:123], v[96:99], v[64:79]\n\t"
"ds_read_b32 v146, v147\n\t"
"v_cvt_pk_bf16_f32 v114, v52, v53\n\t"
"v_exp_f32 v55, v55\n\t"
"v_exp_f32 v56, v56\n\t"
"v_cvt_pk_bf16_f32 v115, v54, v55\n\t"
"v_exp_f32 v57, v57\n\t"
"v_exp_f32 v58, v58\n\t"
"v_mfma_f32_32x32x16_bf16 v[16:31], v[136:139], v[112:115], v[16:31]\n\t"
"v_cvt_pk_bf16_f32 v116, v56, v57\n\t"
"v_exp_f32 v59, v59\n\t"
"v_exp_f32 v60, v60\n\t"
"v_cvt_pk_bf16_f32 v117, v58, v59\n\t"
"v_exp_f32 v61, v61\n\t"
"v_exp_f32 v62, v62\n\t"
"v_exp_f32 v63, v63\n\t"
"v_cvt_pk_bf16_f32 v118, v60, v61\n\t"
"v_cvt_pk_bf16_f32 v119, v62, v63\n\t"
"v_exp_f32 v32, v32\n\t"
"v_exp_f32 v33, v33\n\t"
"v_mfma_f32_32x32x16_bf16 v[16:31], v[140:143], v[116:119], v[16:31]\n\t"
"ds_read_b128 v[132:135], v144 offset:7168\n\t"
"v_exp_f32 v34, v34\n\t"
"v_cvt_pk_bf16_f32 v104, v32, v33\n\t"
"v_exp_f32 v35, v35\n\t"
"v_exp_f32 v36, v36\n\t"
"v_cvt_pk_bf16_f32 v105, v34, v35\n\t"
"v_exp_f32 v37, v37\n\t"
"v_exp_f32 v38, v38\n\t"
"v_mfma_f32_32x32x16_bf16 v[48:63], v[120:123], v[100:103], v[80:95]\n\t"
"ds_read_b128 v[136:139], v145 offset:7616\n\t"
"v_cvt_pk_bf16_f32 v106, v36, v37\n\t"
"v_exp_f32 v39, v39\n\t"
"v_exp_f32 v40, v40\n\t"
"v_cvt_pk_bf16_f32 v107, v38, v39\n\t"
"v_exp_f32 v41, v41\n\t"
"v_exp_f32 v42, v42\n\t"
"v_mfma_f32_32x32x16_bf16 v[0:15], v[124:127], v[104:107], v[0:15]\n\t"
"ds_read_b128 v[140:143], v145 offset:8160\n\t"
"v_cvt_pk_bf16_f32 v108, v40, v41\n\t"
"v_exp_f32 v43, v43\n\t"
"v_exp_f32 v44, v44\n\t"
"v_cvt_pk_bf16_f32 v109, v42, v43\n\t"
"v_exp_f32 v45, v45\n\t"
"v_exp_f32 v46, v46\n\t"
"v_exp_f32 v47, v47\n\t"
"v_cvt_pk_bf16_f32 v110, v44, v45\n\t"
"v_cvt_pk_bf16_f32 v111, v46, v47\n\t"
"v_exp_f32 v48, v48\n\t"
"v_exp_f32 v49, v49\n\t"
"v_mfma_f32_32x32x16_bf16 v[0:15], v[128:131], v[108:111], v[0:15]\n\t"
"s_waitcnt lgkmcnt(0)\n\t"
"v_exp_f32 v50, v50\n\t"
"v_cvt_pk_bf16_f32 v112, v48, v49\n\t"
"v_exp_f32 v51, v51\n\t"
"v_exp_f32 v52, v52\n\t"
"v_cvt_pk_bf16_f32 v113, v50, v51\n\t"
"v_exp_f32 v53, v53\n\t"
"v_exp_f32 v54, v54\n\t"
"v_mfma_f32_32x32x16_bf16 v[32:47], v[132:135], v[96:99], v[64:79]\n\t"
"v_readfirstlane_b32 %1, v146\n\t"
"v_cvt_pk_bf16_f32 v114, v52, v53\n\t"
"v_exp_f32 v55, v55\n\t"
"v_exp_f32 v56, v56\n\t"
"v_cvt_pk_bf16_f32 v115, v54, v55\n\t"
"v_exp_f32 v57, v57\n\t"
"v_exp_f32 v58, v58\n\t"
"v_mfma_f32_32x32x16_bf16 v[16:31], v[124:127], v[112:115], v[16:31]\n\t"
"s_cmp_lt_u32 %1, 4\n\t"
"s_cbranch_scc1 2f\n\t"
"3:\n\t"
"v_cvt_pk_bf16_f32 v116, v56, v57\n\t"
"v_exp_f32 v59, v59\n\t"
"v_exp_f32 v60, v60\n\t"
"v_cvt_pk_bf16_f32 v117, v58, v59\n\t"
"v_exp_f32 v61, v61\n\t"
"v_exp_f32 v62, v62\n\t"
"v_exp_f32 v63, v63\n\t"
"v_cvt_pk_bf16_f32 v118, v60, v61\n\t"
"v_cvt_pk_bf16_f32 v119, v62, v63\n\t"
"v_exp_f32 v32, v32\n\t"
"v_exp_f32 v33, v33\n\t"
"v_mfma_f32_32x32x16_bf16 v[16:31], v[128:131], v[116:119], v[16:31]\n\t"
"ds_read_b128 v[120:123], v144 offset:8192\n\t"
"v_exp_f32 v34, v34\n\t"
"v_cvt_pk_bf16_f32 v104, v32, v33\n\t"
"v_exp_f32 v35, v35\n\t"
"v_exp_f32 v36, v36\n\t"
"v_cvt_pk_bf16_f32 v105, v34, v35\n\t"
"v_exp_f32 v37, v37\n\t"
"v_exp_f32 v38, v38\n\t"
"v_mfma_f32_32x32x16_bf16 v[48:63], v[132:135], v[100:103], v[80:95]\n\t"
"ds_read_b128 v[124:127], v145 offset:8704\n\t"
"v_cvt_pk_bf16_f32 v106, v36, v37\n\t"
"v_exp_f32 v39, v39\n\t"
"v_exp_f32 v40, v40\n\t"
"v_cvt_pk_bf16_f32 v107, v38, v39\n\t"
"v_exp_f32 v41, v41\n\t"
"v_exp_f32 v42, v42\n\t"
"v_mfma_f32_32x32x16_bf16 v[0:15], v[136:139], v[104:107], v[0:15]\n\t"
"ds_read_b128 v[128:131], v145 offset:9248\n\t"
"v_cvt_pk_bf16_f32 v108, v40, v41\n\t"
"v_exp_f32 v43, v43\n\t"
"v_exp_f32 v44, v44\n\t"
"v_cvt_pk_bf16_f32 v109, v42, v43\n\t"
"v_exp_f32 v45, v45\n\t"
"v_exp_f32 v46, v46\n\t"
"v_exp_f32 v47, v47\n\t"
"v_cvt_pk_bf16_f32 v110, v44, v45\n\t"
"v_cvt_pk_bf16_f32 v111, v46, v47\n\t"
"v_exp_f32 v48, v48\n\t"
"v_exp_f32 v49, v49\n\t"
"v_mfma_f32_32x32x16_bf16 v[0:15], v[140:143], v[108:111], v[0:15]\n\t"
"s_waitcnt lgkmcnt(0)\n\t"
"v_exp_f32 v50, v50\n\t"
"v_cvt_pk_bf16_f32 v112, v48, v49\n\t"
"v_exp_f32 v51, v51\n\t"
"v_exp_f32 v52, v52\n\t"
"v_cvt_pk_bf16_f32 v113, v50, v51\n\t"
"v_exp_f32 v53, v53\n\t"
"v_exp_f32 v54, v54\n\t"
"v_mfma_f32_32x32x16_bf16 v[32:47], v[120:123], v[96:99], v[64:79]\n\t"
"s_sub_u32 %0, %0, 1\n\t"
"v_cvt_pk_bf16_f32 v114, v52, v53\n\t"
"v_exp_f32 v55, v55\n\t"
"v_exp_f32 v56, v56\n\t"
"v_cvt_pk_bf16_f32 v115, v54, v55\n\t"
"v_exp_f32 v57, v57\n\t"
"v_exp_f32 v58, v58\n\t"
"v_mfma_f32_32x32x16_bf16 v[16:31], v[136:139], v[112:115], v[16:31]\n\t"
"s_cmp_lg_u32 %0, 0\n\t"
"v_add_u32 v144, 8192, v144\n\t"
"v_add_u32 v145, 8704, v145\n\t"
"v_cvt_pk_bf16_f32 v116, v56, v57\n\t"
"v_exp_f32 v59, v59\n\t"
"v_exp_f32 v60, v60\n\t"
"v_cvt_pk_bf16_f32 v117, v58, v59\n\t"
"v_exp_f32 v61, v61\n\t"
"v_exp_f32 v62, v62\n\t"
"v_exp_f32 v63, v63\n\t"
"v_cvt_pk_bf16_f32 v118, v60, v61\n\t"
"v_cvt_pk_bf16_f32 v119, v62, v63\n\t"
"s_cbranch_scc1 1b\n\t"
"s_branch 4f\n\t"
"2:\n\ts_sleep 1\n\tds_read_b32 v146, v147\n\ts_waitcnt lgkmcnt(0)\n\tv_readfirstlane_b32 %1, v146\n\ts_nop 3\n\ts_cmp_lt_u32 %1, 4\n\ts_cbranch_scc1 2b\n\ts_branch 3b\n\t"
"4:\n\ts_nop 7\n\ts_nop 7"
    : "+s"(n), "=&s"(fl), "={v146}"(fv), "+{v[0:15]}"(s.oa), "+{v[16:31]}"(s.ob), "+{v[32:47]}"(s.sa), "+{v[48:63]}"(s.sb),
      "+{v[104:107]}"(s.pa0), "+{v[108:111]}"(s.pa1), "+{v[112:115]}"(s.pb0), "+{v[116:119]}"(s.pb1),
      "+{v[120:123]}"(s.k0), "+{v[124:127]}"(s.v00), "+{v[128:131]}"(s.v01), "+{v[132:135]}"(s.k1), "+{v[136:139]}"(s.v10), "+{v[140:143]}"(s.v11),
      "+{v144}"(s.kad), "+{v145}"(s.vad)
    : "{v[64:79]}"(s.ca), "{v[80:95]}"(s.cb), "{v[96:99]}"(s.qa), "{v[100:103]}"(s.qb), "{v147}"(flagaddr)
    : "scc", "memory");
}

constexpr int K2_FLG = KV_BYTES;
constexpr int K2_LDS = K2_FLG + 128;

DI void attn_wave(LAS char* lds, char* __restrict__ ws, int head, int qt0, int lane) {
  const int r = lane & 31, h = lane >> 5;
  LAS unsigned* rdy = (LAS unsigned*)(lds + K2_FLG);
  const LAS char* kp = lds + KV_K + lane * 16;
  const LAS char* vp = lds + KV_V + h * 272 + (r < 16 ? r : 16) * 16;
  AttnState s;
  s.qa = *(const bf16x8*)(ws + QF_OFF + ((size_t)head * 64 + qt0) * 1024 + lane * 16);
  s.qb = *(const bf16x8*)(ws + QF_OFF + ((size_t)head * 64 + qt0 + 1) * 1024 + lane * 16);
  asm volatile("s_waitcnt vmcnt(0)" : "+v"(s.qa), "+v"(s.qb) :: "memory");
  lds_signal(rdy + 0, lane);
  lds_wait(rdy + 0, 4);

  float ma = -3.0e38f, mb = -3.0e38f;
#pragma unroll
  for (int t = 0; t < 2; ++t) {
    const bf16x8 kf = lds128(kp + t * 1024);
    ma = fmaxf(ma, max16(MFMA(kf, s.qa, f32x16{})));
    mb = fmaxf(mb, max16(MFMA(kf, s.qb, f32x16{})));
  }
  ma = -(xhalf_max(ma) + M_OFFSET); mb = -(xhalf_max(mb) + M_OFFSET);
#pragma unroll
  for (int i = 0; i < 16; ++i) { s.ca[i] = ma; s.cb[i] = mb; s.oa[i] = 0.f; s.ob[i] = 0.f; s.sb[i] = 0.f; }
  s.k0 = lds128(kp); s.v00 = lds128(vp); s.v01 = lds128(vp + 544); s.k1 = s.k0; s.v10 = s.v00; s.v11 = s.v00;
  s.pa0 = bf16x8{}; s.pa1 = bf16x8{}; s.pb0 = bf16x8{}; s.pb1 = bf16x8{};
  s.sa = MFMA(s.k0, s.qa, s.ca);
  s.kad = (unsigned)(size_t)kp; s.vad = (unsigned)(size_t)vp;

  attn_loop(s, 8, (unsigned)(size_t)(rdy + 1));

  f32x16 oa = s.oa, ob = MFMA(s.v11, s.pb1, s.ob);
  if (__any(bad16(oa))) oa = attn_slow(kp, vp, s.qa);
  if (__any(bad16(ob))) ob = attn_slow(kp, vp, s.qb);
  const rsrc_t ro = mkrsrc(ws + OF_OFF, 64 * 32 * 1024);
  attn_store(ro, (qt0 * 32 + head) * 1024 + lane * 16, oa);
  attn_store(ro, ((qt0 + 1) * 32 + head) * 1024 + lane * 16, ob);
  asm volatile("s_waitcnt vmcnt(0)" ::: "memory");
  if (lane == 0) {
    if (__hip_atomic_fetch_add(rdy + 7, 1u, __ATOMIC_RELAXED, __HIP_MEMORY_SCOPE_WORKGROUP) == 3u)
      __hip_atomic_fetch_add((unsigned*)(ws + CNT_OFF + (32 + (qt0 >> 3)) * 128), 1u, __ATOMIC_RELAXED, __HIP_MEMORY_SCOPE_AGENT);
  }

}

__global__ __launch_bounds__(512, 2) void k2_attn_ffn(const char* __restrict__ x, char* __restrict__ ws, char* __restrict__ out) {
  extern __shared__ __attribute__((aligned(16))) char smem[];
  LAS char* lds = (LAS char*)smem;
  const unsigned lds0 = (unsigned)(size_t)lds;
  const int tid = threadIdx.x, lane = tid & 63, w = __builtin_amdgcn_readfirstlane(tid >> 6), u = w & 3;
  const int b = blockIdx.x, xcd = b & 7, idx = b >> 3;
  const int head = xcd * 4 + (idx >> 3), qb = idx & 7;

  LAS unsigned* rdy = (LAS unsigned*)(lds + K2_FLG);
  if (tid < 16) rdy[tid] = 0;
  const char* ksrc = ws + KF_OFF + (size_t)head * 65536;
  const char* vsrc = ws + VF_OFF + (size_t)head * (64 * VT_BYTES);
  auto glds4 = [&](const char* src, unsigned dst) {
    unsigned sv;
    asm volatile("s_mov_b32 %0, m0\n\ts_mov_b32 m0, %3\n\ts_nop 0\n\tglobal_load_lds_dwordx4 %1, %2\n\tglobal_load_lds_dwordx4 %1, %2 offset:1024\n\t"
                 "global_load_lds_dwordx4 %1, %2 offset:2048\n\tglobal_load_lds_dwordx4 %1, %2 offset:3072\n\ts_mov_b32 m0, %0"
                 : "=&s"(sv) : "v"(lane * 16), "s"(src), "s"(dst) : "memory");
  };
  auto stage = [&](int g) {
    glds4(ksrc + (g * 16 + 4 * u) * 1024, lds0 + KV_K + (g * 16 + 4 * u) * 1024);
    glds4(vsrc + (g * 17 + 4 * u) * 1024, lds0 + KV_V + (g * 17 + 4 * u) * 1024);
    if (u == 0) glds16(lane * 16, vsrc + (g * 17 + 16) * 1024, lds0 + KV_V + (g * 17 + 16) * 1024);
  };
  if (w < 4) stage(0);
  BARL();
  if (__builtin_expect(w < 4, 1)) { attn_wave(lds, ws, head, qb * 8 + 2 * w, lane); return; }

  unsigned pf = code_prefetch((idx * 4 + u) % K2_CODE_KB, lane);
  stage(1); stage(2); stage(3);
  asm volatile("s_waitcnt vmcnt(0)" : "+v"(pf) :: "memory"); GPRIO(1); lds_signal(rdy + 1, lane);

  const int rp = head, cg = qb;
  if (cg < 7) ffn1_wave<2>(ws, 2 * rp, 8 * cg + 2 * u, lane);
  unsigned* cnt = (unsigned*)(ws + CNT_OFF + rp * 128);
  asm volatile("s_waitcnt vmcnt(0)" ::: "memory");
  if (lane == 0) {
    if (__hip_atomic_fetch_add(rdy + 5, 1u, __ATOMIC_RELAXED, __HIP_MEMORY_SCOPE_WORKGROUP) == 3u)
      __hip_atomic_fetch_add(cnt, 1u, __ATOMIC_RELAXED, __HIP_MEMORY_SCOPE_AGENT);
  }

  if (u == 0) {
    for (int spin = 0; spin < (1 << 20); ++spin) {
      if (__builtin_amdgcn_readfirstlane(__hip_atomic_load(cnt, __ATOMIC_RELAXED, __HIP_MEMORY_SCOPE_AGENT)) >= 8u) break;
      __builtin_amdgcn_s_sleep(8);
    }
    lds_signal(rdy + 6, lane);
  } else lds_wait<8>(rdy + 6, 1);
  __builtin_amdgcn_fence(__ATOMIC_ACQUIRE, "wavefront");

  f32x16 acc[2][2];
  gemm_frag<2, 28, 6, 16>(mkrsrc(ws + HF_OFF, 64 * 112 * 1024), ((2 * rp) * 112 + 28 * u) * 1024, 112 * 1024,
                          mkrsrc(ws + W2F_OFF, 16 * 112 * 1024), ((2 * cg) * 112 + 28 * u) * 1024, 112 * 1024, lane * 16, acc);

  const rsrc_t rwo = mkrsrc(ws + WOF_OFF, 16 * 32 * 1024), ro = mkrsrc(ws + OF_OFF, 64 * 32 * 1024);
  bf16x8 wf[8][2];
#pragma unroll
  for (int k = 0; k < 8; ++k) { wf[k][0] = bld<0>(rwo, lane * 16, ((2 * cg) * 32 + 8 * u + k) * 1024); wf[k][1] = bld<0>(rwo, lane * 16, ((2 * cg + 1) * 32 + 8 * u + k) * 1024); }
  const int gt = tid - 256;
  u32x4 xv[2];
#pragma unroll
  for (int e = 0; e < 2; ++e) xv[e] = *(const u32x4*)(x + (size_t)(rp * 64 + e * 32 + (gt >> 3)) * 1024 + cg * 128 + (gt & 7) * 16);
  {
    unsigned* cnt2 = (unsigned*)(ws + CNT_OFF + (32 + (rp >> 2)) * 128);
    lds_wait<8>(rdy + 7, 1);
    for (int spin = 0; spin < (1 << 20); ++spin) {
      if (__builtin_amdgcn_readfirstlane(__hip_atomic_load(cnt2, __ATOMIC_RELAXED, __HIP_MEMORY_SCOPE_AGENT)) >= 32u) break;
      __builtin_amdgcn_s_sleep(1);
    }
  }
  __builtin_amdgcn_fence(__ATOMIC_ACQUIRE, "wavefront");

  {
    bf16x8 of[8][2];
#pragma unroll
    for (int k = 0; k < 8; ++k) { of[k][0] = bld<16>(ro, lane * 16, ((2 * rp) * 32 + 8 * u + k) * 1024); of[k][1] = bld<16>(ro, lane * 16, ((2 * rp + 1) * 32 + 8 * u + k) * 1024); }
    SBAR();
#pragma unroll
    for (int k = 0; k < 8; ++k)
#pragma unroll
      for (int i = 0; i < 2; ++i)
#pragma unroll
        for (int j = 0; j < 2; ++j) acc[i][j] = MFMA(wf[k][j], of[k][i], acc[i][j]);
  }

  lds_wait<1>(rdy + 7, 4);
  dump22(lds + u * 17408, acc, lane & 31, lane >> 5);
  lds_signal(rdy + 9, lane);
  lds_wait<1>(rdy + 9, 4);
#pragma unroll
  for (int e = 0; e < 2; ++e) {
    const int row = e * 32 + (gt >> 3), c8 = gt & 7;
    f32x4 s0 = {}, s1 = {};
#pragma unroll
    for (int v = 0; v < 4; ++v) { const LAS char* p = lds + v * 17408 + row * 272 + c8 * 32; s0 += *(const LAS f32x4*)p; s1 += *(const LAS f32x4*)(p + 16); }
    u32x4 o;
    o[0] = cvtpk(bf_lo(xv[e][0]) + s0[0], bf_hi(xv[e][0]) + s0[1]); o[1] = cvtpk(bf_lo(xv[e][1]) + s0[2], bf_hi(xv[e][1]) + s0[3]);
    o[2] = cvtpk(bf_lo(xv[e][2]) + s1[0], bf_hi(xv[e][2]) + s1[1]); o[3] = cvtpk(bf_lo(xv[e][3]) + s1[2], bf_hi(xv[e][3]) + s1[3]);
    gst(out + (size_t)(rp * 64 + row) * 1024 + cg * 128 + c8 * 16, o);
  }

}

extern "C" void kernel_launch(void* const* d_in, const int* in_sizes, int n_in, void* d_out, int out_size, void* d_ws, size_t ws_size, hipStream_t stream) {
  const char* x = (const char*)d_in[0];
  const char* wqkv = (const char*)d_in[1];
  const char* wo = (const char*)d_in[2];
  const char* w1 = (const char*)d_in[4];
  const char* w2 = (const char*)d_in[5];
  char* ws = (char*)d_ws;
  (void)hipFuncSetAttribute((const void*)k1_qkv, hipFuncAttributeMaxDynamicSharedMemorySize, K1_LDS);
  (void)hipFuncSetAttribute((const void*)k2_attn_ffn, hipFuncAttributeMaxDynamicSharedMemorySize, K2_LDS);
  k1_qkv<<<256, 512, K1_LDS, stream>>>(x, wqkv, wo, w1, w2, ws);
  k2_attn_ffn<<<256, 512, K2_LDS, stream>>>(x, ws, (char*)d_out);
}
```
